```python
import torch

def _softmax(x, axis=-1):
    return torch.softmax(x, dim=axis)
def _gelu(x):
    return torch.nn.functional.gelu(x, approximate="tanh")
def _silu(x):
    return torch.nn.functional.silu(x)
def _sigmoid(x):
    return torch.sigmoid(x)
def _layernorm(x, axis=-1, eps=1e-5):
    m = x.mean(dim=axis, keepdim=True)
    v = x.var(dim=axis, keepdim=True, unbiased=False)
    return (x - m) / torch.sqrt(v + eps)
def _scatter_add(tgt, idx, src, axis=0):
    return tgt.clone().index_add_(axis, idx, src)
def _segment_sum(src, seg, n):
    out = torch.zeros((n,) + src.shape[1:], dtype=src.dtype)
    return out.index_add_(0, seg, src)
def _pad(x, pw):
    flat = tuple(v for pair in reversed(pw) for v in pair)
    return torch.nn.functional.pad(x, flat)

kernel_name = 'standalone__glu_gated_glu__prompt'

def setup_inputs():

    _w = (lambda a: (lambda t: t.to(torch.bfloat16) if t.is_floating_point() else t)(torch.as_tensor(a)))
    B, S, D, F = 1, 2048, 640, 1536
    return {
        "x": _w((lambda *s: torch.randn(*s, dtype=torch.bfloat16) * 0.2)(B, S, D)),
        "w_gate": _w((lambda *s: torch.randn(*s, dtype=torch.bfloat16) * 0.2)(D, F)),
        "w_up": _w((lambda *s: torch.randn(*s, dtype=torch.bfloat16) * 0.05)(D, F)),
        "w_down": _w((lambda *s: torch.randn(*s, dtype=torch.bfloat16) * 0.05)(F, D)),
    }

def reference(x, w_gate, w_up, w_down):
    g = torch.einsum("bsd,df->bsf", x, w_gate)
    u = torch.einsum("bsd,df->bsf", x, w_up)
    h = _sigmoid(g) * u
    return torch.einsum("bsf,fd->bsd", h, w_down)
```

```cpp
#include <hip/hip_runtime.h>
#include <hip/hip_bf16.h>

__global__ __launch_bounds__(512, 2) void glu_fused(const void* x, unsigned nx, unsigned fx, const void* wg, unsigned ng, unsigned fg,
                                                    const void* wu, unsigned nu, unsigned fu, const void* wd, unsigned nd, unsigned fd,
                                                    void* out, unsigned no, unsigned fo, void* ws, unsigned nw, unsigned fw) {
  asm volatile(
    "ENTRY_%=:\n"
    "s_load_dwordx16 s[8:23], s[0:1], 0\n"
    "s_load_dwordx8 s[24:31], s[0:1], 64\n"
    "v_readfirstlane_b32 s3, v0\n"
    "v_and_b32 v1, 63, v0\n"
    "s_lshr_b32 s4, s2, 3\n"
    "s_mul_i32 s5, s4, 43\n"
    "s_lshr_b32 s5, s5, 8\n"
    "s_mul_i32 s48, s5, 6\n"
    "s_sub_u32 s51, s4, s48\n"
    "s_and_b32 s48, s2, 3\n"
    "s_mul_i32 s48, s48, 6\n"
    "s_add_u32 s7, s48, s51\n"
    "s_and_b32 s48, s2, 4\n"
    "s_mul_i32 s34, s5, 13\n"
    "s_lshl4_add_u32 s34, s48, s34\n"
    "s_lshr_b32 s3, s3, 6\n"
    "v_lshlrev_b32 v7, 4, v1\n"
    "s_cmp_ge_u32 s4, 30\n"
    "s_cbranch_scc1 SPAREWG_%=\n"
    "s_sub_u32 s35, s3, 4\n"
    "s_cbranch_scc1 COMPUTE_%=\n"
    "v_lshrrev_b32 v9, 3, v1\n"
    "v_and_b32 v10, 7, v1\n"
    "v_and_b32 v11, 2, v9\n"
    "v_xor_b32 v11, v11, v10\n"
    "v_mul_u32_u24 v136, 3072, v9\n"
    "v_lshl_add_u32 v136, v11, 4, v136\n"
    "s_lshl_b32 s48, s2, 2\n"
    "s_lshr_b32 s39, 0x10434310, s48\n"
    "s_and_b32 s39, s39, 15\n"
    "s_add_u32 s48, s5, s51\n"
    "s_bitcmp1_b32 s48, 0\n"
    "s_cselect_b32 s48, 5, 0\n"
    "s_add_u32 s39, s39, s48\n"
    "v_xor_b32 v137, 64, v136\n"
    "v_add_u32 v137, 23552, v137\n"
    "s_mul_i32 s41, s35, 49152\n"
    "s_lshl_b32 s48, s7, 7\n"
    "s_add_u32 s41, s41, s48\n"
    "s_lshl_b32 s42, s35, 11\n"
    "s_add_u32 s42, s42, 69632\n"
    "s_mul_i32 s48, s35, 36\n"
    "v_add_u32 v9, s48, v9\n"
    "s_movk_i32 s50, 1280\n"
    "v_lshrrev_b32 v11, 1, v9\n"
    "v_xor_b32 v11, v11, v10\n"
    "v_and_b32 v11, 7, v11\n"
    "v_lshlrev_b32 v11, 4, v11\n"
    "v_mad_u32_u24 v148, v9, s50, v11\n"
    "v_xor_b32 v149, 64, v148\n"
    "v_cmp_lt_u32 vcc, 111, v9\n"
    "s_cmp_eq_u32 s35, 3\n"
    "s_cselect_b32 s48, 81920, 0\n"
    "v_add_u32 v150, 18432, v148\n"
    "v_add_u32 v11, 81920, v150\n"
    "v_cndmask_b32 v150, v150, v11, vcc\n"
    "s_add_u32 s49, s48, 27648\n"
    "v_add_u32 v151, s49, v149\n"
    "s_add_u32 s49, s48, 40960\n"
    "v_add_u32 v152, s49, v148\n"
    "v_add_u32 v149, 9216, v149\n"
    "s_mul_i32 s40, s34, 20480\n"
    "s_mul_i32 s51, s35, 2560\n"
    "s_add_u32 s51, s51, 16384\n"
    "s_mov_b32 m0, s42\n"
    "s_mul_i32 s49, s39, 196608\n"
    "s_add_u32 s49, s49, s41\n"
    "s_waitcnt lgkmcnt(0)\n"
    "buffer_load_dwordx4 v136, s[12:15], s49 offen offset:0 lds\n"
    "buffer_load_dwordx4 v137, s[12:15], s49 offen offset:1024 lds\n"
    "s_add_u32 m0, m0, 8192\n"
    "s_nop 0\n"
    "buffer_load_dwordx4 v136, s[16:19], s49 offen offset:0 lds\n"
    "buffer_load_dwordx4 v137, s[16:19], s49 offen offset:1024 lds\n"
    "s_add_u32 m0, s42, s51\n"
    "s_lshl_b32 s49, s39, 7\n"
    "s_add_u32 s49, s49, s40\n"
    "buffer_load_dwordx4 v148, s[8:11], s49 offen offset:0 lds\n"
    "buffer_load_dwordx4 v149, s[8:11], s49 offen offset:1024 lds\n"
    "buffer_load_dwordx4 v150, s[8:11], s49 offen offset:2048 lds\n"
    "buffer_load_dwordx4 v151, s[8:11], s49 offen offset:3072 lds\n"
    "s_add_u32 m0, m0, 4096\n"
    "s_mov_b32 exec_hi, 0\n"
    "buffer_load_dwordx4 v152, s[8:11], s49 offen lds\n"
    "s_mov_b32 exec_hi, -1\n"
    "s_mov_b32 s37, s39\n"
    "s_mov_b32 s48, 8292352\n"
    "s_load_dwordx2 s[32:33], s[28:29], s48\n"
    "s_lshr_b32 s36, s2, 5\n"
    "s_and_b32 s48, s2, 1\n"
    "s_and_b32 s49, s4, 3\n"
    "s_lshl2_add_u32 s6, s48, s49\n"
    "s_and_b32 s48, s2, 4\n"
    "s_lshl1_add_u32 s48, s48, s36\n"
    "s_bfe_u32 s49, s2, 0x10001\n"
    "s_lshl1_add_u32 s64, s48, s49\n"
    "s_and_b32 s59, s3, 1\n"
    "s_lshr_b32 s60, s3, 1\n"
    "s_add_u32 s80, s3, s36\n"
    "s_and_b32 s80, s80, 7\n"
    "s_mul_i32 s50, s64, 192\n"
    "s_mul_i32 s48, s80, 6\n"
    "s_add_u32 s50, s50, s48\n"
    "s_add_u32 s48, s60, 1\n"
    "s_and_b32 s48, s48, 3\n"
    "s_mul_i32 s48, s48, 48\n"
    "s_add_u32 s48, s48, s50\n"
    "s_lshl_b32 s54, s48, 10\n"
    "s_add_u32 s48, s60, 2\n"
    "s_and_b32 s48, s48, 3\n"
    "s_mul_i32 s48, s48, 48\n"
    "s_add_u32 s48, s48, s50\n"
    "s_lshl_b32 s55, s48, 10\n"
    "s_add_u32 s48, s60, 3\n"
    "s_and_b32 s48, s48, 3\n"
    "s_mul_i32 s48, s48, 48\n"
    "s_add_u32 s48, s48, s50\n"
    "s_lshl_b32 s56, s48, 10\n"
    "s_add_u32 s48, s60, 4\n"
    "s_and_b32 s48, s48, 3\n"
    "s_mul_i32 s48, s48, 48\n"
    "s_add_u32 s48, s48, s50\n"
    "s_lshl_b32 s57, s48, 10\n"
    "s_mul_i32 s82, s3, 5120\n"
    "s_xor_b32 s48, s3, 4\n"
    "s_mul_i32 s83, s48, 5120\n"
    "s_add_u32 s48, s3, 2\n"
    "s_and_b32 s48, s48, 7\n"
    "s_mul_i32 s84, s48, 5120\n"
    "s_xor_b32 s48, s48, 1\n"
    "s_mul_i32 s85, s48, 5120\n"
    "s_xor_b32 s48, s3, 1\n"
    "s_mul_i32 s86, s48, 5120\n"
    "s_add_u32 s86, s86, 81920\n"
    "s_lshl2_add_u32 s48, s64, s60\n"
    "s_mul_i32 s48, s48, 20480\n"
    "s_mul_i32 s49, s6, 160\n"
    "s_add_u32 s88, s48, s49\n"
    "s_mul_i32 s49, s59, 96\n"
    "s_add_u32 s87, s88, s49\n"
    "s_lshl3_add_u32 s48, s6, s80\n"
    "s_mul_i32 s58, s48, 30720\n"
    "s_add_u32 s58, s58, 6291456\n"
    "s_lshl_b32 s61, s6, 8\n"
    "s_add_u32 s61, s61, 8290304\n"
    "s_lshl_b32 s48, s59, 12\n"
    "v_add_u32 v240, s48, v7\n"
    "s_lshl_b32 s48, s59, 11\n"
    "s_sub_u32 s48, 1024, s48\n"
    "v_add_u32 v241, s48, v240\n"
    "v_add_u32 v242, s48, v241\n"
    "v_add_u32 v243, s48, v242\n"
    "v_add_u32 v244, s48, v243\n"
    "v_add_u32 v224, s82, v7\n"
    "v_add_u32 v225, 122880, v224\n"
    "v_add_u32 v226, s83, v7\n"
    "v_add_u32 v227, 122880, v226\n"
    "v_add_u32 v228, s84, v7\n"
    "v_add_u32 v229, s85, v7\n"
    "v_add_u32 v233, s86, v7\n"
    "v_and_b32 v9, 15, v1\n"
    "v_mul_u32_u24 v9, 1280, v9\n"
    "v_and_b32 v10, 48, v1\n"
    "v_add_u32 v231, v9, v10\n"
    "v_lshrrev_b32 v10, 1, v10\n"
    "v_add_u32 v232, v9, v10\n"
    "v_add_u32 v8, 4096, v7\n"
    "s_and_b32 s48, s64, 15\n"
    "s_mul_i32 s48, s48, 316\n"
    "s_add_u32 s49, s48, 237\n"
    "s_lshr_b32 s48, s48, 10\n"
    "s_lshr_b32 s49, s49, 10\n"
    "s_sub_u32 s49, s49, s48\n"
    "s_and_b32 s50, s64, 15\n"
    "s_lshr_b32 s48, s64, 4\n"
    "s_lshl_b32 s48, s48, 14\n"
    "s_lshl_b32 s50, s50, 9\n"
    "s_add_u32 s48, s48, s50\n"
    "s_lshl_b32 s49, s49, 3\n"
    "v_and_b32 v9, 31, v1\n"
    "v_min_u32 v9, 23, v9\n"
    "v_lshlrev_b32 v9, 3, v9\n"
    "v_and_b32 v10, 32, v1\n"
    "v_mad_u32_u24 v230, v10, s49, v9\n"
    "v_add_u32 v230, s48, v230\n"
    "s_xor_b32 s48, s80, 4\n"
    "s_lshl3_add_u32 s48, s6, s48\n"
    "s_mul_i32 s93, s48, 30720\n"
    "s_add_u32 s93, s93, 6291456\n"
    "s_mov_b64 vcc, 0\n"
    "v_lshrrev_b32 v9, 3, v1\n"
    "v_lshrrev_b32 v11, 4, v1\n"
    "s_lshl_b32 s48, s35, 2\n"
    "v_add_u32 v11, s48, v11\n"
    "v_xor_b32 v11, v11, v1\n"
    "v_and_b32 v11, 7, v11\n"
    "v_mul_u32_u24 v142, 1280, v9\n"
    "v_lshl_add_u32 v142, v11, 4, v142\n"
    "s_mul_i32 s48, s35, 10240\n"
    "s_add_u32 s48, s48, 163840\n"
    "v_add_u32 v142, s48, v142\n"
    "v_and_b32 v143, 31, v1\n"
    "v_lshlrev_b32 v143, 3, v143\n"
    "s_lshr_b32 s49, s34, 2\n"
    "s_and_b32 s50, s49, 15\n"
    "s_lshr_b32 s48, s49, 4\n"
    "s_lshl_b32 s48, s48, 14\n"
    "s_lshl_b32 s50, s50, 9\n"
    "s_add_u32 s48, s48, s50\n"
    "s_lshl_b32 s49, s7, 3\n"
    "s_add_u32 s48, s48, s49\n"
    "s_add_u32 s62, s48, 8257536\n"
    "v_and_b32 v146, 3, v1\n"
    "v_lshlrev_b32 v146, 9, v146\n"
    "s_and_b32 s48, s34, 3\n"
    "s_cselect_b32 s48, 256, 0\n"
    "v_writelane_b32 v146, s48, 0\n"
    "v_writelane_b32 v146, s48, 4\n"
    "v_and_b32 v9, 4, v1\n"
    "v_lshl_add_u32 v146, v9, 11, v146\n"
    "s_cmp_eq_u32 s5, 4\n"
    "s_cselect_b32 s90, 7, 15\n"
    "s_mul_i32 s90, s90, 17\n"
    "s_mov_b32 s38, 1\n"
    "LLOOP_%=:\n"
    "s_add_u32 s39, s39, 1\n"
    "s_cmp_eq_u32 s39, 10\n"
    "s_cselect_b32 s39, 0, s39\n"
    "s_add_u32 s42, s42, 34816\n"
    "s_cmp_ge_u32 s42, 104448\n"
    "s_cselect_b32 s48, 104448, 0\n"
    "s_sub_u32 s42, s42, s48\n"
    "s_mov_b32 m0, s42\n"
    "s_mul_i32 s49, s39, 196608\n"
    "s_add_u32 s49, s49, s41\n"
    "buffer_load_dwordx4 v136, s[12:15], s49 offen offset:0 lds\n"
    "buffer_load_dwordx4 v137, s[12:15], s49 offen offset:1024 lds\n"
    "s_add_u32 m0, m0, 8192\n"
    "s_nop 0\n"
    "buffer_load_dwordx4 v136, s[16:19], s49 offen offset:0 lds\n"
    "buffer_load_dwordx4 v137, s[16:19], s49 offen offset:1024 lds\n"
    "s_add_u32 m0, s42, s51\n"
    "s_lshl_b32 s49, s39, 7\n"
    "s_add_u32 s49, s49, s40\n"
    "buffer_load_dwordx4 v148, s[8:11], s49 offen offset:0 lds\n"
    "buffer_load_dwordx4 v149, s[8:11], s49 offen offset:1024 lds\n"
    "buffer_load_dwordx4 v150, s[8:11], s49 offen offset:2048 lds\n"
    "buffer_load_dwordx4 v151, s[8:11], s49 offen offset:3072 lds\n"
    "s_add_u32 m0, m0, 4096\n"
    "s_mov_b32 exec_hi, 0\n"
    "buffer_load_dwordx4 v152, s[8:11], s49 offen lds\n"
    "s_mov_b32 exec_hi, -1\n"
    "s_waitcnt vmcnt(9) lgkmcnt(0)\n"
    "s_barrier\n"
    "s_add_u32 s38, s38, 1\n"
    "s_cmp_lt_u32 s38, 10\n"
    "s_cbranch_scc1 LLOOP_%=\n"
    "s_add_u32 s32, s32, 1\n"
    "s_addc_u32 s33, s33, 0\n"
    "s_lshl_b32 s42, s35, 10\n"
    "s_add_u32 m0, s42, 122880\n"
    "s_lshl_b32 s39, s37, 7\n"
    "s_mov_b32 s38, 6\n"
    "XI1_%=:\n"
    "s_add_u32 s49, s39, s40\n"
    "buffer_load_dwordx4 v142, s[8:11], s49 offen lds\n"
    "s_add_u32 m0, m0, 4096\n"
    "s_add_u32 s39, s39, 128\n"
    "s_cmp_eq_u32 s39, 1280\n"
    "s_cselect_b32 s39, 0, s39\n"
    "s_sub_u32 s38, s38, 1\n"
    "s_cmp_lg_u32 s38, 0\n"
    "s_cbranch_scc1 XI1_%=\n"
    "s_waitcnt vmcnt(6)\n"
    "s_barrier\n"
    "s_mov_b32 s38, 4\n"
    "XI2_%=:\n"
    "s_add_u32 s49, s39, s40\n"
    "buffer_load_dwordx4 v142, s[8:11], s49 offen lds\n"
    "s_add_u32 m0, m0, 4096\n"
    "s_add_u32 s39, s39, 128\n"
    "s_cmp_eq_u32 s39, 1280\n"
    "s_cselect_b32 s39, 0, s39\n"
    "s_sub_u32 s38, s38, 1\n"
    "s_cmp_lg_u32 s38, 0\n"
    "s_cbranch_scc1 XI2_%=\n"
    "buffer_load_dwordx2 v[144:145], v143, s[28:31], s61 offen sc1\n"
    "s_add_u32 s40, s40, 40960\n"
    "s_add_u32 m0, s42, 0\n"
    "s_lshl_b32 s39, s37, 7\n"
    "s_mov_b32 s38, 5\n"
    "XI3_%=:\n"
    "s_add_u32 s49, s39, s40\n"
    "buffer_load_dwordx4 v142, s[8:11], s49 offen lds\n"
    "s_add_u32 m0, m0, 4096\n"
    "s_add_u32 s39, s39, 128\n"
    "s_cmp_eq_u32 s39, 1280\n"
    "s_cselect_b32 s39, 0, s39\n"
    "s_sub_u32 s38, s38, 1\n"
    "s_cmp_lg_u32 s38, 0\n"
    "s_cbranch_scc1 XI3_%=\n"
    "s_waitcnt vmcnt(6)\n"
    "s_barrier\n"
    "s_mov_b32 s38, 5\n"
    "XI4_%=:\n"
    "s_add_u32 s49, s39, s40\n"
    "buffer_load_dwordx4 v142, s[8:11], s49 offen lds\n"
    "s_add_u32 m0, m0, 4096\n"
    "s_add_u32 s39, s39, 128\n"
    "s_cmp_eq_u32 s39, 1280\n"
    "s_cselect_b32 s39, 0, s39\n"
    "s_sub_u32 s38, s38, 1\n"
    "s_cmp_lg_u32 s38, 0\n"
    "s_cbranch_scc1 XI4_%=\n"
    "s_waitcnt vmcnt(10)\n"
    "v_cmp_eq_u64 s[66:67], s[32:33], v[144:145]\n"
    "s_cmp_eq_u64 s[66:67], -1\n"
    "s_cbranch_scc0 PRETRY_%=\n"
    "PCOK_%=:\n"
    "buffer_load_dwordx4 v[16:19], v240, s[28:31], s58 offen sc1\n"
    "buffer_load_dwordx4 v[20:23], v241, s[28:31], s58 offen sc1\n"
    "buffer_load_dwordx4 v[24:27], v242, s[28:31], s58 offen sc1\n"
    "buffer_load_dwordx4 v[28:31], v243, s[28:31], s58 offen sc1\n"
    "buffer_load_dwordx4 v[32:35], v244, s[28:31], s58 offen sc1\n"
    "s_add_u32 s58, s58, 5120\n"
    "buffer_load_dwordx4 v[36:39], v240, s[28:31], s58 offen sc1\n"
    "buffer_load_dwordx4 v[40:43], v241, s[28:31], s58 offen sc1\n"
    "buffer_load_dwordx4 v[44:47], v242, s[28:31], s58 offen sc1\n"
    "buffer_load_dwordx4 v[48:51], v243, s[28:31], s58 offen sc1\n"
    "buffer_load_dwordx4 v[52:55], v244, s[28:31], s58 offen sc1\n"
    "s_add_u32 s58, s58, 5120\n"
    "s_waitcnt vmcnt(10)\n"
    "s_barrier\n"
    "buffer_load_dwordx4 v[56:59], v240, s[28:31], s58 offen sc1\n"
    "buffer_load_dwordx4 v[60:63], v241, s[28:31], s58 offen sc1\n"
    "buffer_load_dwordx4 v[64:67], v242, s[28:31], s58 offen sc1\n"
    "buffer_load_dwordx4 v[68:71], v243, s[28:31], s58 offen sc1\n"
    "buffer_load_dwordx4 v[72:75], v244, s[28:31], s58 offen sc1\n"
    "s_add_u32 s58, s58, 5120\n"
    "buffer_load_dwordx4 v[76:79], v240, s[28:31], s58 offen sc1\n"
    "buffer_load_dwordx4 v[80:83], v241, s[28:31], s58 offen sc1\n"
    "buffer_load_dwordx4 v[84:87], v242, s[28:31], s58 offen sc1\n"
    "buffer_load_dwordx4 v[88:91], v243, s[28:31], s58 offen sc1\n"
    "buffer_load_dwordx4 v[92:95], v244, s[28:31], s58 offen sc1\n"
    "s_add_u32 s58, s58, 5120\n"
    "buffer_load_dwordx4 v[96:99], v240, s[28:31], s58 offen sc1\n"
    "buffer_load_dwordx4 v[100:103], v241, s[28:31], s58 offen sc1\n"
    "buffer_load_dwordx4 v[104:107], v242, s[28:31], s58 offen sc1\n"
    "buffer_load_dwordx4 v[108:111], v243, s[28:31], s58 offen sc1\n"
    "buffer_load_dwordx4 v[112:115], v244, s[28:31], s58 offen sc1\n"
    "s_add_u32 s58, s58, 5120\n"
    "buffer_load_dwordx4 v[116:119], v240, s[28:31], s58 offen sc1\n"
    "buffer_load_dwordx4 v[120:123], v241, s[28:31], s58 offen sc1\n"
    "buffer_load_dwordx4 v[124:127], v242, s[28:31], s58 offen sc1\n"
    "buffer_load_dwordx4 v[128:131], v243, s[28:31], s58 offen sc1\n"
    "buffer_load_dwordx4 v[132:135], v244, s[28:31], s58 offen sc1\n"
    "s_cmp_eq_u32 s3, 5\n"
    "s_cbranch_scc1 LPOST_%=\n"
    "LP2_%=:\n"
    "s_cmp_lg_u32 s3, 4\n"
    "s_cbranch_scc1 LBX_%=\n"
    "s_setprio 3\n"
    "s_barrier\n"
    "s_mov_b32 s48, 8292480\n"
    "s_load_dwordx2 s[96:97], s[28:29], s48\n"
    "s_sleep 18\n"
    "HPOLL_%=:\n"
    "s_waitcnt lgkmcnt(0)\n"
    "s_xor_b64 s[96:97], s[96:97], s[32:33]\n"
    "s_lshr_b32 s48, s97, 29\n"
    "s_lshl_b64 s[96:97], s[96:97], 1\n"
    "s_cselect_b32 s48, 0, s48\n"
    "s_getreg_b32 s49, hwreg(HW_REG_XCC_ID, 0, 4)\n"
    "s_xor_b32 s48, s48, s49\n"
    "s_and_b32 s48, s48, 4\n"
    "s_lshl_b32 s48, s48, 11\n"
    "s_add_u32 s48, s48, 8257536\n"
    "s_mov_b32 s45, 1048576\n"
    "PL5_%=:\n"
    "buffer_load_dwordx2 v[236:237], v230, s[28:31], s48 offen sc1\n"
    "s_sub_u32 s45, s45, 1\n"
    "s_cbranch_scc1 PL5x_%=\n"
    "s_waitcnt vmcnt(0)\n"
    "v_cmp_eq_u64 s[66:67], s[32:33], v[236:237]\n"
    "s_cmp_eq_u64 s[66:67], -1\n"
    "s_cbranch_scc1 PL5x_%=\n"
    "s_sleep 2\n"
    "s_branch PL5_%=\n"
    "PL5x_%=:\n"
    "s_setprio 0\n"
    "s_branch HOK_%=\n"
    "LPOST_%=:\n"
    "v_mov_b32 v10, s32\n"
    "v_mov_b32 v11, s33\n"
    "s_setprio 3\n"
    "s_mov_b32 exec_lo, s90\n"
    "s_mov_b32 exec_hi, 0\n"
    "s_barrier\n"
    "buffer_store_dwordx2 v[10:11], v146, s[28:31], s62 offen sc1\n"
    "s_mov_b64 exec, -1\n"
    "s_setprio 0\n"
    "s_branch HOK_%=\n"
    "LBX_%=:\n"
    "s_barrier\n"
    "s_branch HOK_%=\n"
    "PRETRY_%=:\n"
    "s_mov_b32 s45, 1048576\n"
    "PL6_%=:\n"
    "buffer_load_dwordx2 v[144:145], v143, s[28:31], s61 offen sc1\n"
    "s_sub_u32 s45, s45, 1\n"
    "s_cbranch_scc1 PL6x_%=\n"
    "s_waitcnt vmcnt(0)\n"
    "v_cmp_eq_u64 s[66:67], s[32:33], v[144:145]\n"
    "s_cmp_eq_u64 s[66:67], -1\n"
    "s_cbranch_scc1 PL6x_%=\n"
    "s_sleep 2\n"
    "s_branch PL6_%=\n"
    "PL6x_%=:\n"
    "s_branch PCOK_%=\n"
    "COMPUTE_%=:\n"
    "s_mov_b64 vcc, 0\n"
    "s_lshl4_add_u32 s48, s3, s4\n"
    "s_cmp_ge_u32 s48, 16\n"
    "s_cbranch_scc1 NOPACK_%=\n"
    "v_mul_u32_u24 v9, 205, v1\n"
    "s_movk_i32 s48, 1120\n"
    "v_lshrrev_b32 v10, 11, v9\n"
    "v_mad_u32_u24 v156, v10, s48, v7\n"
    "v_add_u32 v10, 13120, v9\n"
    "v_lshrrev_b32 v10, 11, v10\n"
    "v_mad_u32_u24 v157, v10, s48, v7\n"
    "v_add_u32 v10, 26240, v9\n"
    "v_lshrrev_b32 v10, 11, v10\n"
    "v_mad_u32_u24 v158, v10, s48, v7\n"
    "v_add_u32 v10, 39360, v9\n"
    "v_lshrrev_b32 v10, 11, v10\n"
    "v_mad_u32_u24 v159, v10, s48, v7\n"
    "v_add_u32 v10, 52480, v9\n"
    "v_lshrrev_b32 v10, 11, v10\n"
    "v_mad_u32_u24 v160, v10, s48, v7\n"
    "v_add_u32 v160, 4096, v160\n"
    "s_mov_b32 m0, 112640\n"
    "s_and_b32 s50, s2, 7\n"
    "s_add_u32 s39, s4, 32\n"
    "s_mul_i32 s49, s39, 40960\n"
    "s_mul_i32 s48, s50, 160\n"
    "s_add_u32 s49, s49, s48\n"
    "s_waitcnt lgkmcnt(0)\n"
    "buffer_load_dwordx4 v156, s[20:23], s49 offen offset:0 lds\n"
    "buffer_load_dwordx4 v157, s[20:23], s49 offen offset:1024 lds\n"
    "buffer_load_dwordx4 v158, s[20:23], s49 offen offset:2048 lds\n"
    "buffer_load_dwordx4 v159, s[20:23], s49 offen offset:3072 lds\n"
    "s_add_u32 m0, m0, 4096\n"
    "s_nop 0\n"
    "buffer_load_dwordx4 v160, s[20:23], s49 offen lds\n"
    "s_mov_b32 s48, 8292352\n"
    "s_load_dwordx2 s[32:33], s[28:29], s48\n"
    "v_lshrrev_b32 v10, 4, v1\n"
    "v_bfe_u32 v9, v1, 2, 2\n"
    "v_lshl_add_u32 v9, v10, 3, v9\n"
    "v_mul_u32_u24 v9, 160, v9\n"
    "v_add_u32 v9, 112640, v9\n"
    "v_and_b32 v10, 3, v1\n"
    "v_lshl_add_u32 v161, v10, 4, v9\n"
    "v_lshl_add_u32 v162, v10, 3, v9\n"
    "v_add_u32 v156, 4096, v7\n"
    "s_mul_i32 s48, s50, 48\n"
    "s_add_u32 s48, s48, s39\n"
    "s_mul_i32 s40, s48, 5120\n"
    "s_add_u32 s40, s40, 6291456\n"
    "s_lshl_b32 s48, s50, 5\n"
    "s_add_u32 s48, s48, s4\n"
    "s_lshl_b32 s48, s48, 3\n"
    "s_add_u32 s62, s48, 8290432\n"
    "s_mov_b64 vcc, -1\n"
    "NOPACK_%=:\n"
    "s_mov_b64 exec, 0\n"
    "s_cmp_eq_u32 s3, 1\n"
    "s_cbranch_scc1 PAD1_%=\n"
    "s_cmp_eq_u32 s3, 2\n"
    "s_cbranch_scc1 PAD2_%=\n"
    "s_cmp_eq_u32 s3, 3\n"
    "s_cbranch_scc1 PAD3_%=\n"
    "s_branch PAD0_%=\n"
    "PADRET_%=:\n"
    "s_mov_b64 exec, -1\n"
    "s_lshr_b32 s36, s2, 5\n"
    "s_and_b32 s48, s2, 1\n"
    "s_and_b32 s49, s4, 3\n"
    "s_lshl2_add_u32 s6, s48, s49\n"
    "s_and_b32 s48, s2, 4\n"
    "s_lshl1_add_u32 s48, s48, s36\n"
    "s_bfe_u32 s49, s2, 0x10001\n"
    "s_lshl1_add_u32 s64, s48, s49\n"
    "s_and_b32 s59, s3, 1\n"
    "s_lshr_b32 s60, s3, 1\n"
    "s_add_u32 s80, s3, s36\n"
    "s_and_b32 s80, s80, 7\n"
    "s_mul_i32 s50, s64, 192\n"
    "s_mul_i32 s48, s80, 6\n"
    "s_add_u32 s50, s50, s48\n"
    "s_add_u32 s48, s60, 1\n"
    "s_and_b32 s48, s48, 3\n"
    "s_mul_i32 s48, s48, 48\n"
    "s_add_u32 s48, s48, s50\n"
    "s_lshl_b32 s54, s48, 10\n"
    "s_add_u32 s48, s60, 2\n"
    "s_and_b32 s48, s48, 3\n"
    "s_mul_i32 s48, s48, 48\n"
    "s_add_u32 s48, s48, s50\n"
    "s_lshl_b32 s55, s48, 10\n"
    "s_add_u32 s48, s60, 3\n"
    "s_and_b32 s48, s48, 3\n"
    "s_mul_i32 s48, s48, 48\n"
    "s_add_u32 s48, s48, s50\n"
    "s_lshl_b32 s56, s48, 10\n"
    "s_add_u32 s48, s60, 4\n"
    "s_and_b32 s48, s48, 3\n"
    "s_mul_i32 s48, s48, 48\n"
    "s_add_u32 s48, s48, s50\n"
    "s_lshl_b32 s57, s48, 10\n"
    "s_mul_i32 s82, s3, 5120\n"
    "s_xor_b32 s48, s3, 4\n"
    "s_mul_i32 s83, s48, 5120\n"
    "s_add_u32 s48, s3, 2\n"
    "s_and_b32 s48, s48, 7\n"
    "s_mul_i32 s84, s48, 5120\n"
    "s_xor_b32 s48, s48, 1\n"
    "s_mul_i32 s85, s48, 5120\n"
    "s_xor_b32 s48, s3, 1\n"
    "s_mul_i32 s86, s48, 5120\n"
    "s_add_u32 s86, s86, 81920\n"
    "s_lshl2_add_u32 s48, s64, s60\n"
    "s_mul_i32 s48, s48, 20480\n"
    "s_mul_i32 s49, s6, 160\n"
    "s_add_u32 s88, s48, s49\n"
    "s_mul_i32 s49, s59, 96\n"
    "s_add_u32 s87, s88, s49\n"
    "s_lshl3_add_u32 s48, s6, s80\n"
    "s_mul_i32 s58, s48, 30720\n"
    "s_add_u32 s58, s58, 6291456\n"
    "s_lshl_b32 s61, s6, 8\n"
    "s_add_u32 s61, s61, 8290304\n"
    "v_and_b32 v9, 15, v1\n"
    "v_lshrrev_b32 v10, 4, v1\n"
    "v_lshrrev_b32 v11, 1, v9\n"
    "v_xor_b32 v11, v11, v10\n"
    "v_lshlrev_b32 v11, 4, v11\n"
    "v_lshl_add_u32 v2, v9, 7, v11\n"
    "v_and_b32 v11, 1, v10\n"
    "v_mul_u32_u24 v0, 49152, v11\n"
    "v_lshl_add_u32 v0, v9, 4, v0\n"
    "v_lshrrev_b32 v3, 1, v10\n"
    "v_lshl_add_u32 v0, v3, 8, v0\n"
    "v_lshl_add_u32 v8, v11, 30, v0\n"
    "v_lshlrev_b32 v11, 1, v11\n"
    "v_lshrrev_b32 v4, 3, v9\n"
    "v_or_b32 v11, v11, v4\n"
    "v_xor_b32 v11, s3, v11\n"
    "v_lshrrev_b32 v9, 2, v9\n"
    "v_lshl_add_u32 v9, v10, 3, v9\n"
    "v_lshlrev_b32 v9, 7, v9\n"
    "v_lshl_add_u32 v4, v11, 5, v9\n"
    "v_and_b32 v10, 3, v1\n"
    "v_lshl_add_u32 v4, v10, 3, v4\n"
    "v_add_u32 v4, 69632, v4\n"
    "v_add_u32 v2, 69632, v2\n"
    "v_xor_b32 v3, 64, v2\n"
    "s_mov_b32 s52, 0xbfb8aa3b\n"
    "s_mov_b32 s53, 0xbfb8aa3b\n"
    "s_mul_i32 s48, s34, 48\n"
    "s_lshl1_add_u32 s48, s7, s48\n"
    "s_lshl_b32 s48, s48, 10\n"
    "s_lshl_b32 s49, s3, 9\n"
    "s_add_u32 s90, s48, s49\n"
    "s_add_u32 s91, s90, 589824\n"
    "s_cmp_eq_u32 s5, 4\n"
    "s_cselect_b32 s91, 1073741824, s91\n"
    "s_waitcnt lgkmcnt(0)\n"
    "s_branch PAD0_%=\n"
    "SPAREWG_%=:\n"
    "v_mul_u32_u24 v9, 205, v1\n"
    "s_movk_i32 s48, 1120\n"
    "v_lshrrev_b32 v10, 11, v9\n"
    "v_mad_u32_u24 v16, v10, s48, v7\n"
    "v_add_u32 v10, 13120, v9\n"
    "v_lshrrev_b32 v10, 11, v10\n"
    "v_mad_u32_u24 v17, v10, s48, v7\n"
    "v_add_u32 v10, 26240, v9\n"
    "v_lshrrev_b32 v10, 11, v10\n"
    "v_mad_u32_u24 v18, v10, s48, v7\n"
    "v_add_u32 v10, 39360, v9\n"
    "v_lshrrev_b32 v10, 11, v10\n"
    "v_mad_u32_u24 v19, v10, s48, v7\n"
    "v_add_u32 v10, 52480, v9\n"
    "v_lshrrev_b32 v10, 11, v10\n"
    "v_mad_u32_u24 v20, v10, s48, v7\n"
    "v_add_u32 v20, 4096, v20\n"
    "s_sub_u32 s39, s2, 240\n"
    "s_mul_i32 s42, s3, 10240\n"
    "s_mov_b32 m0, s42\n"
    "s_mul_i32 s49, s39, 81920\n"
    "s_mul_i32 s48, s3, 160\n"
    "s_add_u32 s49, s49, s48\n"
    "s_waitcnt lgkmcnt(0)\n"
    "buffer_load_dwordx4 v16, s[20:23], s49 offen offset:0 lds\n"
    "buffer_load_dwordx4 v17, s[20:23], s49 offen offset:1024 lds\n"
    "buffer_load_dwordx4 v18, s[20:23], s49 offen offset:2048 lds\n"
    "buffer_load_dwordx4 v19, s[20:23], s49 offen offset:3072 lds\n"
    "s_add_u32 m0, m0, 4096\n"
    "s_nop 0\n"
    "buffer_load_dwordx4 v20, s[20:23], s49 offen lds\n"
    "s_add_u32 m0, m0, 1024\n"
    "s_add_u32 s49, s49, 40960\n"
    "buffer_load_dwordx4 v16, s[20:23], s49 offen offset:0 lds\n"
    "buffer_load_dwordx4 v17, s[20:23], s49 offen offset:1024 lds\n"
    "buffer_load_dwordx4 v18, s[20:23], s49 offen offset:2048 lds\n"
    "buffer_load_dwordx4 v19, s[20:23], s49 offen offset:3072 lds\n"
    "s_add_u32 m0, m0, 4096\n"
    "s_nop 0\n"
    "buffer_load_dwordx4 v20, s[20:23], s49 offen lds\n"
    "s_branch SPARE2_%=\n"
    ".p2align 12\n"
    "PAD0_%=:\n"
    "s_cbranch_execz PADRET_%=\n"
    "s_barrier\n"
    "ds_read_b64_tr_b16 v[32:33], v4 offset:0\n"
    "ds_read_b64_tr_b16 v[34:35], v4 offset:512\n"
    "ds_read_b64_tr_b16 v[36:37], v4 offset:8192\n"
    "ds_read_b64_tr_b16 v[38:39], v4 offset:8704\n"
    "ds_read_b128 v[12:15], v2 offset:16384\n"
    "ds_read_b128 v[16:19], v2 offset:18432\n"
    "ds_read_b128 v[20:23], v2 offset:20480\n"
    "ds_read_b128 v[24:27], v2 offset:22528\n"
    "ds_read_b128 v[28:31], v2 offset:24576\n"
    "s_waitcnt lgkmcnt(4)\n"
    "v_mfma_f32_16x16x32_bf16 v[192:195], v[32:35], v[12:15], 0\n"
    "v_mfma_f32_16x16x32_bf16 v[196:199], v[36:39], v[12:15], 0\n"
    "ds_read_b128 v[12:15], v2 offset:26624\n"
    "s_waitcnt lgkmcnt(4)\n"
    "v_mfma_f32_16x16x32_bf16 v[200:203], v[32:35], v[16:19], 0\n"
    "v_mfma_f32_16x16x32_bf16 v[204:207], v[36:39], v[16:19], 0\n"
    "ds_read_b128 v[16:19], v2 offset:28672\n"
    "s_waitcnt lgkmcnt(4)\n"
    "v_mfma_f32_16x16x32_bf16 v[208:211], v[32:35], v[20:23], 0\n"
    "v_mfma_f32_16x16x32_bf16 v[212:215], v[36:39], v[20:23], 0\n"
    "ds_read_b128 v[20:23], v2 offset:30720\n"
    "s_waitcnt lgkmcnt(4)\n"
    "v_mfma_f32_16x16x32_bf16 v[216:219], v[32:35], v[24:27], 0\n"
    "v_mfma_f32_16x16x32_bf16 v[220:223], v[36:39], v[24:27], 0\n"
    "ds_read_b128 v[24:27], v2 offset:32768\n"
    "s_waitcnt lgkmcnt(4)\n"
    "v_mfma_f32_16x16x32_bf16 v[224:227], v[32:35], v[28:31], 0\n"
    "ds_read_b64_tr_b16 v[40:41], v4 offset:4096\n"
    "v_mfma_f32_16x16x32_bf16 v[228:231], v[36:39], v[28:31], 0\n"
    "ds_read_b128 v[28:31], v3 offset:16384\n"
    "s_waitcnt lgkmcnt(5)\n"
    "v_mfma_f32_16x16x32_bf16 v[232:235], v[32:35], v[12:15], 0\n"
    "ds_read_b64_tr_b16 v[42:43], v4 offset:4608\n"
    "v_mfma_f32_16x16x32_bf16 v[236:239], v[36:39], v[12:15], 0\n"
    "ds_read_b128 v[12:15], v3 offset:18432\n"
    "s_waitcnt lgkmcnt(6)\n"
    "v_mfma_f32_16x16x32_bf16 v[240:243], v[32:35], v[16:19], 0\n"
    "ds_read_b64_tr_b16 v[44:45], v4 offset:12288\n"
    "v_mfma_f32_16x16x32_bf16 v[244:247], v[36:39], v[16:19], 0\n"
    "ds_read_b128 v[16:19], v3 offset:20480\n"
    "s_waitcnt lgkmcnt(7)\n"
    "v_mfma_f32_16x16x32_bf16 v[248:251], v[32:35], v[20:23], 0\n"
    "ds_read_b64_tr_b16 v[46:47], v4 offset:12800\n"
    "v_mfma_f32_16x16x32_bf16 v[252:255], v[36:39], v[20:23], 0\n"
    "ds_read_b128 v[20:23], v3 offset:22528\n"
    "s_waitcnt lgkmcnt(8)\n"
    "v_mfma_f32_16x16x32_bf16 v[184:187], v[32:35], v[24:27], 0\n"
    "v_mfma_f32_16x16x32_bf16 v[188:191], v[36:39], v[24:27], 0\n"
    "ds_read_b128 v[24:27], v3 offset:24576\n"
    "s_waitcnt lgkmcnt(2)\n"
    "v_mfma_f32_16x16x32_bf16 v[192:195], v[40:43], v[28:31], v[192:195]\n"
    "s_lshl_b32 s48, s3, 11\n"
    "s_add_u32 s48, s48, 104448\n"
    "v_add_u32 v9, s48, v7\n"
    "ds_write_b128 v9, v[32:35]\n"
    "ds_write_b128 v9, v[36:39] offset:1024\n"
    "v_mfma_f32_16x16x32_bf16 v[196:199], v[44:47], v[28:31], v[196:199]\n"
    "ds_read_b128 v[28:31], v3 offset:26624\n"
    "v_mfma_f32_16x16x32_bf16 v[200:203], v[40:43], v[12:15], v[200:203]\n"
    "v_mfma_f32_16x16x32_bf16 v[204:207], v[44:47], v[12:15], v[204:207]\n"
    "ds_read_b128 v[12:15], v3 offset:28672\n"
    "v_mfma_f32_16x16x32_bf16 v[208:211], v[40:43], v[16:19], v[208:211]\n"
    "v_mfma_f32_16x16x32_bf16 v[212:215], v[44:47], v[16:19], v[212:215]\n"
    "ds_read_b128 v[16:19], v3 offset:30720\n"
    "s_waitcnt lgkmcnt(6)\n"
    "v_mfma_f32_16x16x32_bf16 v[216:219], v[40:43], v[20:23], v[216:219]\n"
    "v_mfma_f32_16x16x32_bf16 v[220:223], v[44:47], v[20:23], v[220:223]\n"
    "ds_read_b128 v[20:23], v3 offset:32768\n"
    "s_waitcnt lgkmcnt(6)\n"
    "v_mfma_f32_16x16x32_bf16 v[224:227], v[40:43], v[24:27], v[224:227]\n"
    "v_mfma_f32_16x16x32_bf16 v[228:231], v[44:47], v[24:27], v[228:231]\n"
    "s_waitcnt lgkmcnt(3)\n"
    "v_mfma_f32_16x16x32_bf16 v[232:235], v[40:43], v[28:31], v[232:235]\n"
    "s_cbranch_vccnz HOOK1_%=\n"
    "HBACK1_%=:\n"
    "s_waitcnt lgkmcnt(0)\n"
    "s_barrier\n"
    "v_subrev_u32 v4, 69632, v4\n"
    "ds_read_b64_tr_b16 v[48:49], v4 offset:0\n"
    "ds_read_b64_tr_b16 v[50:51], v4 offset:512\n"
    "v_subrev_u32 v2, 69632, v2\n"
    "v_subrev_u32 v3, 69632, v3\n"
    "ds_read_b128 v[24:27], v2 offset:16384\n"
    "ds_read_b64_tr_b16 v[52:53], v4 offset:8192\n"
    "ds_read_b64_tr_b16 v[54:55], v4 offset:8704\n"
    "v_mfma_f32_16x16x32_bf16 v[236:239], v[44:47], v[28:31], v[236:239]\n"
    "ds_read_b128 v[28:31], v2 offset:18432\n"
    "v_mfma_f32_16x16x32_bf16 v[240:243], v[40:43], v[12:15], v[240:243]\n"
    "v_mfma_f32_16x16x32_bf16 v[244:247], v[44:47], v[12:15], v[244:247]\n"
    "ds_read_b128 v[12:15], v2 offset:20480\n"
    "v_mfma_f32_16x16x32_bf16 v[248:251], v[40:43], v[16:19], v[248:251]\n"
    "v_mfma_f32_16x16x32_bf16 v[252:255], v[44:47], v[16:19], v[252:255]\n"
    "ds_read_b128 v[16:19], v2 offset:22528\n"
    "v_mfma_f32_16x16x32_bf16 v[184:187], v[40:43], v[20:23], v[184:187]\n"
    "v_mfma_f32_16x16x32_bf16 v[188:191], v[44:47], v[20:23], v[188:191]\n"
    "ds_read_b128 v[20:23], v2 offset:24576\n"
    "s_waitcnt lgkmcnt(4)\n"
    "v_mfma_f32_16x16x32_bf16 v[192:195], v[48:51], v[24:27], v[192:195]\n"
    "v_mfma_f32_16x16x32_bf16 v[196:199], v[52:55], v[24:27], v[196:199]\n"
    "ds_read_b128 v[24:27], v2 offset:26624\n"
    "s_waitcnt lgkmcnt(4)\n"
    "v_mfma_f32_16x16x32_bf16 v[200:203], v[48:51], v[28:31], v[200:203]\n"
    "v_mfma_f32_16x16x32_bf16 v[204:207], v[52:55], v[28:31], v[204:207]\n"
    "ds_read_b128 v[28:31], v2 offset:28672\n"
    "s_waitcnt lgkmcnt(4)\n"
    "v_mfma_f32_16x16x32_bf16 v[208:211], v[48:51], v[12:15], v[208:211]\n"
    "v_mfma_f32_16x16x32_bf16 v[212:215], v[52:55], v[12:15], v[212:215]\n"
    "ds_read_b128 v[12:15], v2 offset:30720\n"
    "s_waitcnt lgkmcnt(4)\n"
    "v_mfma_f32_16x16x32_bf16 v[216:219], v[48:51], v[16:19], v[216:219]\n"
    "v_mfma_f32_16x16x32_bf16 v[220:223], v[52:55], v[16:19], v[220:223]\n"
    "ds_read_b128 v[16:19], v2 offset:32768\n"
    "s_waitcnt lgkmcnt(4)\n"
    "v_mfma_f32_16x16x32_bf16 v[224:227], v[48:51], v[20:23], v[224:227]\n"
    "ds_read_b64_tr_b16 v[56:57], v4 offset:4096\n"
    "v_mfma_f32_16x16x32_bf16 v[228:231], v[52:55], v[20:23], v[228:231]\n"
    "ds_read_b128 v[20:23], v3 offset:16384\n"
    "s_waitcnt lgkmcnt(5)\n"
    "v_mfma_f32_16x16x32_bf16 v[232:235], v[48:51], v[24:27], v[232:235]\n"
    "ds_read_b64_tr_b16 v[58:59], v4 offset:4608\n"
    "v_mfma_f32_16x16x32_bf16 v[236:239], v[52:55], v[24:27], v[236:239]\n"
    "ds_read_b128 v[24:27], v3 offset:18432\n"
    "s_waitcnt lgkmcnt(6)\n"
    "v_mfma_f32_16x16x32_bf16 v[240:243], v[48:51], v[28:31], v[240:243]\n"
    "ds_read_b64_tr_b16 v[60:61], v4 offset:12288\n"
    "v_mfma_f32_16x16x32_bf16 v[244:247], v[52:55], v[28:31], v[244:247]\n"
    "ds_read_b128 v[28:31], v3 offset:20480\n"
    "s_waitcnt lgkmcnt(7)\n"
    "v_mfma_f32_16x16x32_bf16 v[248:251], v[48:51], v[12:15], v[248:251]\n"
    "ds_read_b64_tr_b16 v[62:63], v4 offset:12800\n"
    "v_mfma_f32_16x16x32_bf16 v[252:255], v[52:55], v[12:15], v[252:255]\n"
    "ds_read_b128 v[12:15], v3 offset:22528\n"
    "s_waitcnt lgkmcnt(8)\n"
    "v_mfma_f32_16x16x32_bf16 v[184:187], v[48:51], v[16:19], v[184:187]\n"
    "v_mfma_f32_16x16x32_bf16 v[188:191], v[52:55], v[16:19], v[188:191]\n"
    "ds_read_b128 v[16:19], v3 offset:24576\n"
    "s_waitcnt lgkmcnt(2)\n"
    "v_mfma_f32_16x16x32_bf16 v[192:195], v[56:59], v[20:23], v[192:195]\n"
    "v_mfma_f32_16x16x32_bf16 v[196:199], v[60:63], v[20:23], v[196:199]\n"
    "ds_read_b128 v[20:23], v3 offset:26624\n"
    "v_mfma_f32_16x16x32_bf16 v[200:203], v[56:59], v[24:27], v[200:203]\n"
    "v_mfma_f32_16x16x32_bf16 v[204:207], v[60:63], v[24:27], v[204:207]\n"
    "ds_read_b128 v[24:27], v3 offset:28672\n"
    "v_mfma_f32_16x16x32_bf16 v[208:211], v[56:59], v[28:31], v[208:211]\n"
    "v_mfma_f32_16x16x32_bf16 v[212:215], v[60:63], v[28:31], v[212:215]\n"
    "ds_read_b128 v[28:31], v3 offset:30720\n"
    "s_waitcnt lgkmcnt(4)\n"
    "v_mfma_f32_16x16x32_bf16 v[216:219], v[56:59], v[12:15], v[216:219]\n"
    "v_mfma_f32_16x16x32_bf16 v[220:223], v[60:63], v[12:15], v[220:223]\n"
    "ds_read_b128 v[12:15], v3 offset:32768\n"
    "s_waitcnt lgkmcnt(4)\n"
    "v_mfma_f32_16x16x32_bf16 v[224:227], v[56:59], v[16:19], v[224:227]\n"
    "v_mfma_f32_16x16x32_bf16 v[228:231], v[60:63], v[16:19], v[228:231]\n"
    "s_waitcnt lgkmcnt(3)\n"
    "v_mfma_f32_16x16x32_bf16 v[232:235], v[56:59], v[20:23], v[232:235]\n"
    "s_waitcnt lgkmcnt(0)\n"
    "s_barrier\n"
    "v_add_u32 v4, 34816, v4\n"
    "ds_read_b64_tr_b16 v[64:65], v4 offset:0\n"
    "ds_read_b64_tr_b16 v[66:67], v4 offset:512\n"
    "v_add_u32 v2, 34816, v2\n"
    "v_add_u32 v3, 34816, v3\n"
    "ds_read_b128 v[16:19], v2 offset:16384\n"
    "ds_read_b64_tr_b16 v[68:69], v4 offset:8192\n"
    "ds_read_b64_tr_b16 v[70:71], v4 offset:8704\n"
    "v_mfma_f32_16x16x32_bf16 v[236:239], v[60:63], v[20:23], v[236:239]\n"
    "ds_read_b128 v[20:23], v2 offset:18432\n"
    "v_mfma_f32_16x16x32_bf16 v[240:243], v[56:59], v[24:27], v[240:243]\n"
    "v_mfma_f32_16x16x32_bf16 v[244:247], v[60:63], v[24:27], v[244:247]\n"
    "ds_read_b128 v[24:27], v2 offset:20480\n"
    "v_mfma_f32_16x16x32_bf16 v[248:251], v[56:59], v[28:31], v[248:251]\n"
    "v_mfma_f32_16x16x32_bf16 v[252:255], v[60:63], v[28:31], v[252:255]\n"
    "ds_read_b128 v[28:31], v2 offset:22528\n"
    "v_mfma_f32_16x16x32_bf16 v[184:187], v[56:59], v[12:15], v[184:187]\n"
    "v_mfma_f32_16x16x32_bf16 v[188:191], v[60:63], v[12:15], v[188:191]\n"
    "ds_read_b128 v[12:15], v2 offset:24576\n"
    "s_waitcnt lgkmcnt(4)\n"
    "v_mfma_f32_16x16x32_bf16 v[192:195], v[64:67], v[16:19], v[192:195]\n"
    "v_mfma_f32_16x16x32_bf16 v[196:199], v[68:71], v[16:19], v[196:199]\n"
    "ds_read_b128 v[16:19], v2 offset:26624\n"
    "s_waitcnt lgkmcnt(4)\n"
    "v_mfma_f32_16x16x32_bf16 v[200:203], v[64:67], v[20:23], v[200:203]\n"
    "v_mfma_f32_16x16x32_bf16 v[204:207], v[68:71], v[20:23], v[204:207]\n"
    "ds_read_b128 v[20:23], v2 offset:28672\n"
    "s_waitcnt lgkmcnt(4)\n"
    "v_mfma_f32_16x16x32_bf16 v[208:211], v[64:67], v[24:27], v[208:211]\n"
    "v_mfma_f32_16x16x32_bf16 v[212:215], v[68:71], v[24:27], v[212:215]\n"
    "ds_read_b128 v[24:27], v2 offset:30720\n"
    "s_waitcnt lgkmcnt(4)\n"
    "v_mfma_f32_16x16x32_bf16 v[216:219], v[64:67], v[28:31], v[216:219]\n"
    "v_mfma_f32_16x16x32_bf16 v[220:223], v[68:71], v[28:31], v[220:223]\n"
    "ds_read_b128 v[28:31], v2 offset:32768\n"
    "s_waitcnt lgkmcnt(4)\n"
    "v_mfma_f32_16x16x32_bf16 v[224:227], v[64:67], v[12:15], v[224:227]\n"
    "ds_read_b64_tr_b16 v[72:73], v4 offset:4096\n"
    "v_mfma_f32_16x16x32_bf16 v[228:231], v[68:71], v[12:15], v[228:231]\n"
    "ds_read_b128 v[12:15], v3 offset:16384\n"
    "s_waitcnt lgkmcnt(5)\n"
    "v_mfma_f32_16x16x32_bf16 v[232:235], v[64:67], v[16:19], v[232:235]\n"
    "ds_read_b64_tr_b16 v[74:75], v4 offset:4608\n"
    "v_mfma_f32_16x16x32_bf16 v[236:239], v[68:71], v[16:19], v[236:239]\n"
    "ds_read_b128 v[16:19], v3 offset:18432\n"
    "s_waitcnt lgkmcnt(6)\n"
    "v_mfma_f32_16x16x32_bf16 v[240:243], v[64:67], v[20:23], v[240:243]\n"
    "ds_read_b64_tr_b16 v[76:77], v4 offset:12288\n"
    "v_mfma_f32_16x16x32_bf16 v[244:247], v[68:71], v[20:23], v[244:247]\n"
    "ds_read_b128 v[20:23], v3 offset:20480\n"
    "s_waitcnt lgkmcnt(7)\n"
    "v_mfma_f32_16x16x32_bf16 v[248:251], v[64:67], v[24:27], v[248:251]\n"
    "ds_read_b64_tr_b16 v[78:79], v4 offset:12800\n"
    "v_mfma_f32_16x16x32_bf16 v[252:255], v[68:71], v[24:27], v[252:255]\n"
    "ds_read_b128 v[24:27], v3 offset:22528\n"
    "s_waitcnt lgkmcnt(8)\n"
    "v_mfma_f32_16x16x32_bf16 v[184:187], v[64:67], v[28:31], v[184:187]\n"
    "v_mfma_f32_16x16x32_bf16 v[188:191], v[68:71], v[28:31], v[188:191]\n"
    "ds_read_b128 v[28:31], v3 offset:24576\n"
    "s_waitcnt lgkmcnt(2)\n"
    "v_mfma_f32_16x16x32_bf16 v[192:195], v[72:75], v[12:15], v[192:195]\n"
    "v_mfma_f32_16x16x32_bf16 v[196:199], v[76:79], v[12:15], v[196:199]\n"
    "ds_read_b128 v[12:15], v3 offset:26624\n"
    "v_mfma_f32_16x16x32_bf16 v[200:203], v[72:75], v[16:19], v[200:203]\n"
    "v_mfma_f32_16x16x32_bf16 v[204:207], v[76:79], v[16:19], v[204:207]\n"
    "ds_read_b128 v[16:19], v3 offset:28672\n"
    "v_mfma_f32_16x16x32_bf16 v[208:211], v[72:75], v[20:23], v[208:211]\n"
    "v_mfma_f32_16x16x32_bf16 v[212:215], v[76:79], v[20:23], v[212:215]\n"
    "ds_read_b128 v[20:23], v3 offset:30720\n"
    "s_waitcnt lgkmcnt(4)\n"
    "v_mfma_f32_16x16x32_bf16 v[216:219], v[72:75], v[24:27], v[216:219]\n"
    "v_mfma_f32_16x16x32_bf16 v[220:223], v[76:79], v[24:27], v[220:223]\n"
    "ds_read_b128 v[24:27], v3 offset:32768\n"
    "s_waitcnt lgkmcnt(4)\n"
    "v_mfma_f32_16x16x32_bf16 v[224:227], v[72:75], v[28:31], v[224:227]\n"
    "v_mfma_f32_16x16x32_bf16 v[228:231], v[76:79], v[28:31], v[228:231]\n"
    "s_waitcnt lgkmcnt(3)\n"
    "v_mfma_f32_16x16x32_bf16 v[232:235], v[72:75], v[12:15], v[232:235]\n"
    "s_waitcnt lgkmcnt(0)\n"
    "s_barrier\n"
    "v_add_u32 v4, 34816, v4\n"
    "ds_read_b64_tr_b16 v[80:81], v4 offset:0\n"
    "ds_read_b64_tr_b16 v[82:83], v4 offset:512\n"
    "v_add_u32 v2, 34816, v2\n"
    "v_add_u32 v3, 34816, v3\n"
    "ds_read_b128 v[28:31], v2 offset:16384\n"
    "ds_read_b64_tr_b16 v[84:85], v4 offset:8192\n"
    "ds_read_b64_tr_b16 v[86:87], v4 offset:8704\n"
    "v_mfma_f32_16x16x32_bf16 v[236:239], v[76:79], v[12:15], v[236:239]\n"
    "ds_read_b128 v[12:15], v2 offset:18432\n"
    "v_mfma_f32_16x16x32_bf16 v[240:243], v[72:75], v[16:19], v[240:243]\n"
    "v_mfma_f32_16x16x32_bf16 v[244:247], v[76:79], v[16:19], v[244:247]\n"
    "ds_read_b128 v[16:19], v2 offset:20480\n"
    "v_mfma_f32_16x16x32_bf16 v[248:251], v[72:75], v[20:23], v[248:251]\n"
    "v_mfma_f32_16x16x32_bf16 v[252:255], v[76:79], v[20:23], v[252:255]\n"
    "ds_read_b128 v[20:23], v2 offset:22528\n"
    "v_mfma_f32_16x16x32_bf16 v[184:187], v[72:75], v[24:27], v[184:187]\n"
    "v_mfma_f32_16x16x32_bf16 v[188:191], v[76:79], v[24:27], v[188:191]\n"
    "ds_read_b128 v[24:27], v2 offset:24576\n"
    "s_waitcnt lgkmcnt(4)\n"
    "v_mfma_f32_16x16x32_bf16 v[192:195], v[80:83], v[28:31], v[192:195]\n"
    "v_mfma_f32_16x16x32_bf16 v[196:199], v[84:87], v[28:31], v[196:199]\n"
    "ds_read_b128 v[28:31], v2 offset:26624\n"
    "s_waitcnt lgkmcnt(4)\n"
    "v_mfma_f32_16x16x32_bf16 v[200:203], v[80:83], v[12:15], v[200:203]\n"
    "v_mfma_f32_16x16x32_bf16 v[204:207], v[84:87], v[12:15], v[204:207]\n"
    "ds_read_b128 v[12:15], v2 offset:28672\n"
    "s_waitcnt lgkmcnt(4)\n"
    "v_mfma_f32_16x16x32_bf16 v[208:211], v[80:83], v[16:19], v[208:211]\n"
    "v_mfma_f32_16x16x32_bf16 v[212:215], v[84:87], v[16:19], v[212:215]\n"
    "ds_read_b128 v[16:19], v2 offset:30720\n"
    "s_waitcnt lgkmcnt(4)\n"
    "v_mfma_f32_16x16x32_bf16 v[216:219], v[80:83], v[20:23], v[216:219]\n"
    "v_mfma_f32_16x16x32_bf16 v[220:223], v[84:87], v[20:23], v[220:223]\n"
    "ds_read_b128 v[20:23], v2 offset:32768\n"
    "s_waitcnt lgkmcnt(4)\n"
    "v_mfma_f32_16x16x32_bf16 v[224:227], v[80:83], v[24:27], v[224:227]\n"
    "ds_read_b64_tr_b16 v[88:89], v4 offset:4096\n"
    "v_mfma_f32_16x16x32_bf16 v[228:231], v[84:87], v[24:27], v[228:231]\n"
    "ds_read_b128 v[24:27], v3 offset:16384\n"
    "s_waitcnt lgkmcnt(5)\n"
    "v_mfma_f32_16x16x32_bf16 v[232:235], v[80:83], v[28:31], v[232:235]\n"
    "ds_read_b64_tr_b16 v[90:91], v4 offset:4608\n"
    "v_mfma_f32_16x16x32_bf16 v[236:239], v[84:87], v[28:31], v[236:239]\n"
    "ds_read_b128 v[28:31], v3 offset:18432\n"
    "s_waitcnt lgkmcnt(6)\n"
    "v_mfma_f32_16x16x32_bf16 v[240:243], v[80:83], v[12:15], v[240:243]\n"
    "ds_read_b64_tr_b16 v[92:93], v4 offset:12288\n"
    "v_mfma_f32_16x16x32_bf16 v[244:247], v[84:87], v[12:15], v[244:247]\n"
    "ds_read_b128 v[12:15], v3 offset:20480\n"
    "s_waitcnt lgkmcnt(7)\n"
    "v_mfma_f32_16x16x32_bf16 v[248:251], v[80:83], v[16:19], v[248:251]\n"
    "ds_read_b64_tr_b16 v[94:95], v4 offset:12800\n"
    "v_mfma_f32_16x16x32_bf16 v[252:255], v[84:87], v[16:19], v[252:255]\n"
    "ds_read_b128 v[16:19], v3 offset:22528\n"
    "s_waitcnt lgkmcnt(8)\n"
    "v_mfma_f32_16x16x32_bf16 v[184:187], v[80:83], v[20:23], v[184:187]\n"
    "v_mfma_f32_16x16x32_bf16 v[188:191], v[84:87], v[20:23], v[188:191]\n"
    "ds_read_b128 v[20:23], v3 offset:24576\n"
    "s_waitcnt lgkmcnt(2)\n"
    "v_mfma_f32_16x16x32_bf16 v[192:195], v[88:91], v[24:27], v[192:195]\n"
    "v_mfma_f32_16x16x32_bf16 v[196:199], v[92:95], v[24:27], v[196:199]\n"
    "ds_read_b128 v[24:27], v3 offset:26624\n"
    "v_mfma_f32_16x16x32_bf16 v[200:203], v[88:91], v[28:31], v[200:203]\n"
    "v_mfma_f32_16x16x32_bf16 v[204:207], v[92:95], v[28:31], v[204:207]\n"
    "ds_read_b128 v[28:31], v3 offset:28672\n"
    "v_mfma_f32_16x16x32_bf16 v[208:211], v[88:91], v[12:15], v[208:211]\n"
    "v_mfma_f32_16x16x32_bf16 v[212:215], v[92:95], v[12:15], v[212:215]\n"
    "ds_read_b128 v[12:15], v3 offset:30720\n"
    "s_waitcnt lgkmcnt(4)\n"
    "v_mfma_f32_16x16x32_bf16 v[216:219], v[88:91], v[16:19], v[216:219]\n"
    "v_mfma_f32_16x16x32_bf16 v[220:223], v[92:95], v[16:19], v[220:223]\n"
    "ds_read_b128 v[16:19], v3 offset:32768\n"
    "s_waitcnt lgkmcnt(4)\n"
    "v_mfma_f32_16x16x32_bf16 v[224:227], v[88:91], v[20:23], v[224:227]\n"
    "v_mfma_f32_16x16x32_bf16 v[228:231], v[92:95], v[20:23], v[228:231]\n"
    "s_waitcnt lgkmcnt(3)\n"
    "v_mfma_f32_16x16x32_bf16 v[232:235], v[88:91], v[24:27], v[232:235]\n"
    "s_waitcnt lgkmcnt(0)\n"
    "s_barrier\n"
    "v_subrev_u32 v4, 69632, v4\n"
    "ds_read_b64_tr_b16 v[96:97], v4 offset:0\n"
    "ds_read_b64_tr_b16 v[98:99], v4 offset:512\n"
    "v_subrev_u32 v2, 69632, v2\n"
    "v_subrev_u32 v3, 69632, v3\n"
    "ds_read_b128 v[20:23], v2 offset:16384\n"
    "ds_read_b64_tr_b16 v[100:101], v4 offset:8192\n"
    "ds_read_b64_tr_b16 v[102:103], v4 offset:8704\n"
    "v_mfma_f32_16x16x32_bf16 v[236:239], v[92:95], v[24:27], v[236:239]\n"
    "ds_read_b128 v[24:27], v2 offset:18432\n"
    "v_mfma_f32_16x16x32_bf16 v[240:243], v[88:91], v[28:31], v[240:243]\n"
    "v_mfma_f32_16x16x32_bf16 v[244:247], v[92:95], v[28:31], v[244:247]\n"
    "ds_read_b128 v[28:31], v2 offset:20480\n"
    "v_mfma_f32_16x16x32_bf16 v[248:251], v[88:91], v[12:15], v[248:251]\n"
    "v_mfma_f32_16x16x32_bf16 v[252:255], v[92:95], v[12:15], v[252:255]\n"
    "ds_read_b128 v[12:15], v2 offset:22528\n"
    "v_mfma_f32_16x16x32_bf16 v[184:187], v[88:91], v[16:19], v[184:187]\n"
    "v_mfma_f32_16x16x32_bf16 v[188:191], v[92:95], v[16:19], v[188:191]\n"
    "ds_read_b128 v[16:19], v2 offset:24576\n"
    "s_waitcnt lgkmcnt(4)\n"
    "v_mfma_f32_16x16x32_bf16 v[192:195], v[96:99], v[20:23], v[192:195]\n"
    "v_mfma_f32_16x16x32_bf16 v[196:199], v[100:103], v[20:23], v[196:199]\n"
    "ds_read_b128 v[20:23], v2 offset:26624\n"
    "s_waitcnt lgkmcnt(4)\n"
    "v_mfma_f32_16x16x32_bf16 v[200:203], v[96:99], v[24:27], v[200:203]\n"
    "v_mfma_f32_16x16x32_bf16 v[204:207], v[100:103], v[24:27], v[204:207]\n"
    "ds_read_b128 v[24:27], v2 offset:28672\n"
    "s_waitcnt lgkmcnt(4)\n"
    "v_mfma_f32_16x16x32_bf16 v[208:211], v[96:99], v[28:31], v[208:211]\n"
    "v_mfma_f32_16x16x32_bf16 v[212:215], v[100:103], v[28:31], v[212:215]\n"
    "ds_read_b128 v[28:31], v2 offset:30720\n"
    "s_waitcnt lgkmcnt(4)\n"
    "v_mfma_f32_16x16x32_bf16 v[216:219], v[96:99], v[12:15], v[216:219]\n"
    "v_mfma_f32_16x16x32_bf16 v[220:223], v[100:103], v[12:15], v[220:223]\n"
    "ds_read_b128 v[12:15], v2 offset:32768\n"
    "s_waitcnt lgkmcnt(4)\n"
    "v_mfma_f32_16x16x32_bf16 v[224:227], v[96:99], v[16:19], v[224:227]\n"
    "ds_read_b64_tr_b16 v[104:105], v4 offset:4096\n"
    "v_mfma_f32_16x16x32_bf16 v[228:231], v[100:103], v[16:19], v[228:231]\n"
    "ds_read_b128 v[16:19], v3 offset:16384\n"
    "s_waitcnt lgkmcnt(5)\n"
    "v_mfma_f32_16x16x32_bf16 v[232:235], v[96:99], v[20:23], v[232:235]\n"
    "ds_read_b64_tr_b16 v[106:107], v4 offset:4608\n"
    "v_mfma_f32_16x16x32_bf16 v[236:239], v[100:103], v[20:23], v[236:239]\n"
    "ds_read_b128 v[20:23], v3 offset:18432\n"
    "s_waitcnt lgkmcnt(6)\n"
    "v_mfma_f32_16x16x32_bf16 v[240:243], v[96:99], v[24:27], v[240:243]\n"
    "ds_read_b64_tr_b16 v[108:109], v4 offset:12288\n"
    "v_mfma_f32_16x16x32_bf16 v[244:247], v[100:103], v[24:27], v[244:247]\n"
    "ds_read_b128 v[24:27], v3 offset:20480\n"
    "s_waitcnt lgkmcnt(7)\n"
    "v_mfma_f32_16x16x32_bf16 v[248:251], v[96:99], v[28:31], v[248:251]\n"
    "ds_read_b64_tr_b16 v[110:111], v4 offset:12800\n"
    "v_mfma_f32_16x16x32_bf16 v[252:255], v[100:103], v[28:31], v[252:255]\n"
    "ds_read_b128 v[28:31], v3 offset:22528\n"
    "s_waitcnt lgkmcnt(8)\n"
    "v_mfma_f32_16x16x32_bf16 v[184:187], v[96:99], v[12:15], v[184:187]\n"
    "v_mfma_f32_16x16x32_bf16 v[188:191], v[100:103], v[12:15], v[188:191]\n"
    "ds_read_b128 v[12:15], v3 offset:24576\n"
    "s_waitcnt lgkmcnt(2)\n"
    "v_mfma_f32_16x16x32_bf16 v[192:195], v[104:107], v[16:19], v[192:195]\n"
    "v_mfma_f32_16x16x32_bf16 v[196:199], v[108:111], v[16:19], v[196:199]\n"
    "ds_read_b128 v[16:19], v3 offset:26624\n"
    "v_mfma_f32_16x16x32_bf16 v[200:203], v[104:107], v[20:23], v[200:203]\n"
    "v_mfma_f32_16x16x32_bf16 v[204:207], v[108:111], v[20:23], v[204:207]\n"
    "ds_read_b128 v[20:23], v3 offset:28672\n"
    "v_mfma_f32_16x16x32_bf16 v[208:211], v[104:107], v[24:27], v[208:211]\n"
    "v_mfma_f32_16x16x32_bf16 v[212:215], v[108:111], v[24:27], v[212:215]\n"
    "ds_read_b128 v[24:27], v3 offset:30720\n"
    "s_waitcnt lgkmcnt(4)\n"
    "v_mfma_f32_16x16x32_bf16 v[216:219], v[104:107], v[28:31], v[216:219]\n"
    "v_mfma_f32_16x16x32_bf16 v[220:223], v[108:111], v[28:31], v[220:223]\n"
    "ds_read_b128 v[28:31], v3 offset:32768\n"
    "s_waitcnt lgkmcnt(4)\n"
    "v_mfma_f32_16x16x32_bf16 v[224:227], v[104:107], v[12:15], v[224:227]\n"
    "v_mfma_f32_16x16x32_bf16 v[228:231], v[108:111], v[12:15], v[228:231]\n"
    "s_waitcnt lgkmcnt(3)\n"
    "v_mfma_f32_16x16x32_bf16 v[232:235], v[104:107], v[16:19], v[232:235]\n"
    "s_cbranch_vccnz HOOK2_%=\n"
    "HBACK2_%=:\n"
    "s_waitcnt lgkmcnt(0)\n"
    "s_barrier\n"
    "v_add_u32 v4, 34816, v4\n"
    "ds_read_b64_tr_b16 v[112:113], v4 offset:0\n"
    "ds_read_b64_tr_b16 v[114:115], v4 offset:512\n"
    "v_add_u32 v2, 34816, v2\n"
    "v_add_u32 v3, 34816, v3\n"
    "ds_read_b128 v[12:15], v2 offset:16384\n"
    "ds_read_b64_tr_b16 v[116:117], v4 offset:8192\n"
    "ds_read_b64_tr_b16 v[118:119], v4 offset:8704\n"
    "v_mfma_f32_16x16x32_bf16 v[236:239], v[108:111], v[16:19], v[236:239]\n"
    "ds_read_b128 v[16:19], v2 offset:18432\n"
    "v_mfma_f32_16x16x32_bf16 v[240:243], v[104:107], v[20:23], v[240:243]\n"
    "v_mfma_f32_16x16x32_bf16 v[244:247], v[108:111], v[20:23], v[244:247]\n"
    "ds_read_b128 v[20:23], v2 offset:20480\n"
    "v_mfma_f32_16x16x32_bf16 v[248:251], v[104:107], v[24:27], v[248:251]\n"
    "v_mfma_f32_16x16x32_bf16 v[252:255], v[108:111], v[24:27], v[252:255]\n"
    "ds_read_b128 v[24:27], v2 offset:22528\n"
    "v_mfma_f32_16x16x32_bf16 v[184:187], v[104:107], v[28:31], v[184:187]\n"
    "v_mfma_f32_16x16x32_bf16 v[188:191], v[108:111], v[28:31], v[188:191]\n"
    "ds_read_b128 v[28:31], v2 offset:24576\n"
    "s_waitcnt lgkmcnt(4)\n"
    "v_mfma_f32_16x16x32_bf16 v[192:195], v[112:115], v[12:15], v[192:195]\n"
    "v_mfma_f32_16x16x32_bf16 v[196:199], v[116:119], v[12:15], v[196:199]\n"
    "ds_read_b128 v[12:15], v2 offset:26624\n"
    "s_waitcnt lgkmcnt(4)\n"
    "v_mfma_f32_16x16x32_bf16 v[200:203], v[112:115], v[16:19], v[200:203]\n"
    "v_mfma_f32_16x16x32_bf16 v[204:207], v[116:119], v[16:19], v[204:207]\n"
    "ds_read_b128 v[16:19], v2 offset:28672\n"
    "s_waitcnt lgkmcnt(4)\n"
    "v_mfma_f32_16x16x32_bf16 v[208:211], v[112:115], v[20:23], v[208:211]\n"
    "v_mfma_f32_16x16x32_bf16 v[212:215], v[116:119], v[20:23], v[212:215]\n"
    "ds_read_b128 v[20:23], v2 offset:30720\n"
    "s_waitcnt lgkmcnt(4)\n"
    "v_mfma_f32_16x16x32_bf16 v[216:219], v[112:115], v[24:27], v[216:219]\n"
    "v_mfma_f32_16x16x32_bf16 v[220:223], v[116:119], v[24:27], v[220:223]\n"
    "ds_read_b128 v[24:27], v2 offset:32768\n"
    "s_waitcnt lgkmcnt(4)\n"
    "v_mfma_f32_16x16x32_bf16 v[224:227], v[112:115], v[28:31], v[224:227]\n"
    "ds_read_b64_tr_b16 v[120:121], v4 offset:4096\n"
    "v_mfma_f32_16x16x32_bf16 v[228:231], v[116:119], v[28:31], v[228:231]\n"
    "ds_read_b128 v[28:31], v3 offset:16384\n"
    "s_waitcnt lgkmcnt(5)\n"
    "v_mfma_f32_16x16x32_bf16 v[232:235], v[112:115], v[12:15], v[232:235]\n"
    "ds_read_b64_tr_b16 v[122:123], v4 offset:4608\n"
    "v_mfma_f32_16x16x32_bf16 v[236:239], v[116:119], v[12:15], v[236:239]\n"
    "ds_read_b128 v[12:15], v3 offset:18432\n"
    "s_waitcnt lgkmcnt(6)\n"
    "v_mfma_f32_16x16x32_bf16 v[240:243], v[112:115], v[16:19], v[240:243]\n"
    "ds_read_b64_tr_b16 v[124:125], v4 offset:12288\n"
    "v_mfma_f32_16x16x32_bf16 v[244:247], v[116:119], v[16:19], v[244:247]\n"
    "ds_read_b128 v[16:19], v3 offset:20480\n"
    "s_waitcnt lgkmcnt(7)\n"
    "v_mfma_f32_16x16x32_bf16 v[248:251], v[112:115], v[20:23], v[248:251]\n"
    "ds_read_b64_tr_b16 v[126:127], v4 offset:12800\n"
    "v_mfma_f32_16x16x32_bf16 v[252:255], v[116:119], v[20:23], v[252:255]\n"
    "ds_read_b128 v[20:23], v3 offset:22528\n"
    "s_waitcnt lgkmcnt(8)\n"
    "v_mfma_f32_16x16x32_bf16 v[184:187], v[112:115], v[24:27], v[184:187]\n"
    "v_mfma_f32_16x16x32_bf16 v[188:191], v[116:119], v[24:27], v[188:191]\n"
    "ds_read_b128 v[24:27], v3 offset:24576\n"
    "s_waitcnt lgkmcnt(2)\n"
    "v_mfma_f32_16x16x32_bf16 v[192:195], v[120:123], v[28:31], v[192:195]\n"
    "v_mfma_f32_16x16x32_bf16 v[196:199], v[124:127], v[28:31], v[196:199]\n"
    "ds_read_b128 v[28:31], v3 offset:26624\n"
    "v_mfma_f32_16x16x32_bf16 v[200:203], v[120:123], v[12:15], v[200:203]\n"
    "v_mfma_f32_16x16x32_bf16 v[204:207], v[124:127], v[12:15], v[204:207]\n"
    "ds_read_b128 v[12:15], v3 offset:28672\n"
    "v_mfma_f32_16x16x32_bf16 v[208:211], v[120:123], v[16:19], v[208:211]\n"
    "v_mfma_f32_16x16x32_bf16 v[212:215], v[124:127], v[16:19], v[212:215]\n"
    "ds_read_b128 v[16:19], v3 offset:30720\n"
    "s_waitcnt lgkmcnt(4)\n"
    "v_mfma_f32_16x16x32_bf16 v[216:219], v[120:123], v[20:23], v[216:219]\n"
    "v_mfma_f32_16x16x32_bf16 v[220:223], v[124:127], v[20:23], v[220:223]\n"
    "ds_read_b128 v[20:23], v3 offset:32768\n"
    "s_waitcnt lgkmcnt(4)\n"
    "v_mfma_f32_16x16x32_bf16 v[224:227], v[120:123], v[24:27], v[224:227]\n"
    "v_mfma_f32_16x16x32_bf16 v[228:231], v[124:127], v[24:27], v[228:231]\n"
    "s_waitcnt lgkmcnt(3)\n"
    "v_mfma_f32_16x16x32_bf16 v[232:235], v[120:123], v[28:31], v[232:235]\n"
    "s_waitcnt lgkmcnt(0)\n"
    "s_barrier\n"
    "v_add_u32 v4, 34816, v4\n"
    "ds_read_b64_tr_b16 v[128:129], v4 offset:0\n"
    "ds_read_b64_tr_b16 v[130:131], v4 offset:512\n"
    "v_add_u32 v2, 34816, v2\n"
    "v_add_u32 v3, 34816, v3\n"
    "ds_read_b128 v[24:27], v2 offset:16384\n"
    "ds_read_b64_tr_b16 v[132:133], v4 offset:8192\n"
    "ds_read_b64_tr_b16 v[134:135], v4 offset:8704\n"
    "v_mfma_f32_16x16x32_bf16 v[236:239], v[124:127], v[28:31], v[236:239]\n"
    "ds_read_b128 v[28:31], v2 offset:18432\n"
    "v_mfma_f32_16x16x32_bf16 v[240:243], v[120:123], v[12:15], v[240:243]\n"
    "v_mfma_f32_16x16x32_bf16 v[244:247], v[124:127], v[12:15], v[244:247]\n"
    "ds_read_b128 v[12:15], v2 offset:20480\n"
    "v_mfma_f32_16x16x32_bf16 v[248:251], v[120:123], v[16:19], v[248:251]\n"
    "v_mfma_f32_16x16x32_bf16 v[252:255], v[124:127], v[16:19], v[252:255]\n"
    "ds_read_b128 v[16:19], v2 offset:22528\n"
    "v_mfma_f32_16x16x32_bf16 v[184:187], v[120:123], v[20:23], v[184:187]\n"
    "v_mfma_f32_16x16x32_bf16 v[188:191], v[124:127], v[20:23], v[188:191]\n"
    "ds_read_b128 v[20:23], v2 offset:24576\n"
    "s_waitcnt lgkmcnt(4)\n"
    "v_mfma_f32_16x16x32_bf16 v[192:195], v[128:131], v[24:27], v[192:195]\n"
    "v_mfma_f32_16x16x32_bf16 v[196:199], v[132:135], v[24:27], v[196:199]\n"
    "ds_read_b128 v[24:27], v2 offset:26624\n"
    "s_waitcnt lgkmcnt(4)\n"
    "v_mfma_f32_16x16x32_bf16 v[200:203], v[128:131], v[28:31], v[200:203]\n"
    "v_mfma_f32_16x16x32_bf16 v[204:207], v[132:135], v[28:31], v[204:207]\n"
    "ds_read_b128 v[28:31], v2 offset:28672\n"
    "s_waitcnt lgkmcnt(4)\n"
    "v_mfma_f32_16x16x32_bf16 v[208:211], v[128:131], v[12:15], v[208:211]\n"
    "v_mfma_f32_16x16x32_bf16 v[212:215], v[132:135], v[12:15], v[212:215]\n"
    "ds_read_b128 v[12:15], v2 offset:30720\n"
    "s_waitcnt lgkmcnt(4)\n"
    "v_mfma_f32_16x16x32_bf16 v[216:219], v[128:131], v[16:19], v[216:219]\n"
    "v_mfma_f32_16x16x32_bf16 v[220:223], v[132:135], v[16:19], v[220:223]\n"
    "ds_read_b128 v[16:19], v2 offset:32768\n"
    "s_waitcnt lgkmcnt(4)\n"
    "v_mfma_f32_16x16x32_bf16 v[224:227], v[128:131], v[20:23], v[224:227]\n"
    "ds_read_b64_tr_b16 v[136:137], v4 offset:4096\n"
    "v_mfma_f32_16x16x32_bf16 v[228:231], v[132:135], v[20:23], v[228:231]\n"
    "ds_read_b128 v[20:23], v3 offset:16384\n"
    "s_waitcnt lgkmcnt(5)\n"
    "v_mfma_f32_16x16x32_bf16 v[232:235], v[128:131], v[24:27], v[232:235]\n"
    "ds_read_b64_tr_b16 v[138:139], v4 offset:4608\n"
    "v_mfma_f32_16x16x32_bf16 v[236:239], v[132:135], v[24:27], v[236:239]\n"
    "ds_read_b128 v[24:27], v3 offset:18432\n"
    "s_waitcnt lgkmcnt(6)\n"
    "v_mfma_f32_16x16x32_bf16 v[240:243], v[128:131], v[28:31], v[240:243]\n"
    "ds_read_b64_tr_b16 v[140:141], v4 offset:12288\n"
    "v_mfma_f32_16x16x32_bf16 v[244:247], v[132:135], v[28:31], v[244:247]\n"
    "ds_read_b128 v[28:31], v3 offset:20480\n"
    "s_waitcnt lgkmcnt(7)\n"
    "v_mfma_f32_16x16x32_bf16 v[248:251], v[128:131], v[12:15], v[248:251]\n"
    "ds_read_b64_tr_b16 v[142:143], v4 offset:12800\n"
    "v_mfma_f32_16x16x32_bf16 v[252:255], v[132:135], v[12:15], v[252:255]\n"
    "ds_read_b128 v[12:15], v3 offset:22528\n"
    "s_waitcnt lgkmcnt(8)\n"
    "v_mfma_f32_16x16x32_bf16 v[184:187], v[128:131], v[16:19], v[184:187]\n"
    "v_mfma_f32_16x16x32_bf16 v[188:191], v[132:135], v[16:19], v[188:191]\n"
    "ds_read_b128 v[16:19], v3 offset:24576\n"
    "s_waitcnt lgkmcnt(2)\n"
    "v_mfma_f32_16x16x32_bf16 v[192:195], v[136:139], v[20:23], v[192:195]\n"
    "v_mfma_f32_16x16x32_bf16 v[196:199], v[140:143], v[20:23], v[196:199]\n"
    "ds_read_b128 v[20:23], v3 offset:26624\n"
    "v_mfma_f32_16x16x32_bf16 v[200:203], v[136:139], v[24:27], v[200:203]\n"
    "v_mfma_f32_16x16x32_bf16 v[204:207], v[140:143], v[24:27], v[204:207]\n"
    "ds_read_b128 v[24:27], v3 offset:28672\n"
    "v_mfma_f32_16x16x32_bf16 v[208:211], v[136:139], v[28:31], v[208:211]\n"
    "v_mfma_f32_16x16x32_bf16 v[212:215], v[140:143], v[28:31], v[212:215]\n"
    "ds_read_b128 v[28:31], v3 offset:30720\n"
    "s_waitcnt lgkmcnt(4)\n"
    "v_mfma_f32_16x16x32_bf16 v[216:219], v[136:139], v[12:15], v[216:219]\n"
    "v_mfma_f32_16x16x32_bf16 v[220:223], v[140:143], v[12:15], v[220:223]\n"
    "ds_read_b128 v[12:15], v3 offset:32768\n"
    "s_waitcnt lgkmcnt(4)\n"
    "v_mfma_f32_16x16x32_bf16 v[224:227], v[136:139], v[16:19], v[224:227]\n"
    "v_mfma_f32_16x16x32_bf16 v[228:231], v[140:143], v[16:19], v[228:231]\n"
    "s_waitcnt lgkmcnt(3)\n"
    "v_mfma_f32_16x16x32_bf16 v[232:235], v[136:139], v[20:23], v[232:235]\n"
    "s_waitcnt lgkmcnt(0)\n"
    "s_branch PAD1_%=\n"
    ".p2align 12\n"
    "PAD1_%=:\n"
    "s_cbranch_execz PADRET_%=\n"
    "s_barrier\n"
    "v_subrev_u32 v4, 69632, v4\n"
    "ds_read_b64_tr_b16 v[144:145], v4 offset:0\n"
    "ds_read_b64_tr_b16 v[146:147], v4 offset:512\n"
    "v_subrev_u32 v2, 69632, v2\n"
    "v_subrev_u32 v3, 69632, v3\n"
    "ds_read_b128 v[16:19], v2 offset:16384\n"
    "ds_read_b64_tr_b16 v[148:149], v4 offset:8192\n"
    "ds_read_b64_tr_b16 v[150:151], v4 offset:8704\n"
    "v_mfma_f32_16x16x32_bf16 v[236:239], v[140:143], v[20:23], v[236:239]\n"
    "ds_read_b128 v[20:23], v2 offset:18432\n"
    "v_mfma_f32_16x16x32_bf16 v[240:243], v[136:139], v[24:27], v[240:243]\n"
    "v_mfma_f32_16x16x32_bf16 v[244:247], v[140:143], v[24:27], v[244:247]\n"
    "ds_read_b128 v[24:27], v2 offset:20480\n"
    "v_mfma_f32_16x16x32_bf16 v[248:251], v[136:139], v[28:31], v[248:251]\n"
    "v_mfma_f32_16x16x32_bf16 v[252:255], v[140:143], v[28:31], v[252:255]\n"
    "ds_read_b128 v[28:31], v2 offset:22528\n"
    "v_mfma_f32_16x16x32_bf16 v[184:187], v[136:139], v[12:15], v[184:187]\n"
    "v_mfma_f32_16x16x32_bf16 v[188:191], v[140:143], v[12:15], v[188:191]\n"
    "ds_read_b128 v[12:15], v2 offset:24576\n"
    "s_waitcnt lgkmcnt(4)\n"
    "v_mfma_f32_16x16x32_bf16 v[192:195], v[144:147], v[16:19], v[192:195]\n"
    "v_mfma_f32_16x16x32_bf16 v[196:199], v[148:151], v[16:19], v[196:199]\n"
    "ds_read_b128 v[16:19], v2 offset:26624\n"
    "s_waitcnt lgkmcnt(4)\n"
    "v_mfma_f32_16x16x32_bf16 v[200:203], v[144:147], v[20:23], v[200:203]\n"
    "v_mfma_f32_16x16x32_bf16 v[204:207], v[148:151], v[20:23], v[204:207]\n"
    "ds_read_b128 v[20:23], v2 offset:28672\n"
    "s_waitcnt lgkmcnt(4)\n"
    "v_mfma_f32_16x16x32_bf16 v[208:211], v[144:147], v[24:27], v[208:211]\n"
    "v_mfma_f32_16x16x32_bf16 v[212:215], v[148:151], v[24:27], v[212:215]\n"
    "ds_read_b128 v[24:27], v2 offset:30720\n"
    "s_waitcnt lgkmcnt(4)\n"
    "v_mfma_f32_16x16x32_bf16 v[216:219], v[144:147], v[28:31], v[216:219]\n"
    "v_mfma_f32_16x16x32_bf16 v[220:223], v[148:151], v[28:31], v[220:223]\n"
    "ds_read_b128 v[28:31], v2 offset:32768\n"
    "s_waitcnt lgkmcnt(4)\n"
    "v_mfma_f32_16x16x32_bf16 v[224:227], v[144:147], v[12:15], v[224:227]\n"
    "ds_read_b64_tr_b16 v[152:153], v4 offset:4096\n"
    "v_mfma_f32_16x16x32_bf16 v[228:231], v[148:151], v[12:15], v[228:231]\n"
    "ds_read_b128 v[12:15], v3 offset:16384\n"
    "s_waitcnt lgkmcnt(5)\n"
    "v_mfma_f32_16x16x32_bf16 v[232:235], v[144:147], v[16:19], v[232:235]\n"
    "ds_read_b64_tr_b16 v[154:155], v4 offset:4608\n"
    "v_mfma_f32_16x16x32_bf16 v[236:239], v[148:151], v[16:19], v[236:239]\n"
    "ds_read_b128 v[16:19], v3 offset:18432\n"
    "s_waitcnt lgkmcnt(6)\n"
    "v_mfma_f32_16x16x32_bf16 v[240:243], v[144:147], v[20:23], v[240:243]\n"
    "ds_read_b64_tr_b16 v[156:157], v4 offset:12288\n"
    "v_mfma_f32_16x16x32_bf16 v[244:247], v[148:151], v[20:23], v[244:247]\n"
    "ds_read_b128 v[20:23], v3 offset:20480\n"
    "s_waitcnt lgkmcnt(7)\n"
    "v_mfma_f32_16x16x32_bf16 v[248:251], v[144:147], v[24:27], v[248:251]\n"
    "ds_read_b64_tr_b16 v[158:159], v4 offset:12800\n"
    "v_mfma_f32_16x16x32_bf16 v[252:255], v[148:151], v[24:27], v[252:255]\n"
    "ds_read_b128 v[24:27], v3 offset:22528\n"
    "s_waitcnt lgkmcnt(8)\n"
    "v_mfma_f32_16x16x32_bf16 v[184:187], v[144:147], v[28:31], v[184:187]\n"
    "v_mfma_f32_16x16x32_bf16 v[188:191], v[148:151], v[28:31], v[188:191]\n"
    "ds_read_b128 v[28:31], v3 offset:24576\n"
    "s_waitcnt lgkmcnt(2)\n"
    "v_mfma_f32_16x16x32_bf16 v[192:195], v[152:155], v[12:15], v[192:195]\n"
    "v_mfma_f32_16x16x32_bf16 v[196:199], v[156:159], v[12:15], v[196:199]\n"
    "ds_read_b128 v[12:15], v3 offset:26624\n"
    "v_mfma_f32_16x16x32_bf16 v[200:203], v[152:155], v[16:19], v[200:203]\n"
    "v_mfma_f32_16x16x32_bf16 v[204:207], v[156:159], v[16:19], v[204:207]\n"
    "ds_read_b128 v[16:19], v3 offset:28672\n"
    "v_mfma_f32_16x16x32_bf16 v[208:211], v[152:155], v[20:23], v[208:211]\n"
    "v_mfma_f32_16x16x32_bf16 v[212:215], v[156:159], v[20:23], v[212:215]\n"
    "ds_read_b128 v[20:23], v3 offset:30720\n"
    "s_waitcnt lgkmcnt(4)\n"
    "v_mfma_f32_16x16x32_bf16 v[216:219], v[152:155], v[24:27], v[216:219]\n"
    "v_mfma_f32_16x16x32_bf16 v[220:223], v[156:159], v[24:27], v[220:223]\n"
    "ds_read_b128 v[24:27], v3 offset:32768\n"
    "s_waitcnt lgkmcnt(4)\n"
    "v_mfma_f32_16x16x32_bf16 v[224:227], v[152:155], v[28:31], v[224:227]\n"
    "v_mfma_f32_16x16x32_bf16 v[228:231], v[156:159], v[28:31], v[228:231]\n"
    "s_waitcnt lgkmcnt(3)\n"
    "v_mfma_f32_16x16x32_bf16 v[232:235], v[152:155], v[12:15], v[232:235]\n"
    "s_waitcnt lgkmcnt(0)\n"
    "s_barrier\n"
    "v_add_u32 v4, 34816, v4\n"
    "ds_read_b64_tr_b16 v[160:161], v4 offset:0\n"
    "ds_read_b64_tr_b16 v[162:163], v4 offset:512\n"
    "v_add_u32 v2, 34816, v2\n"
    "v_add_u32 v3, 34816, v3\n"
    "ds_read_b128 v[28:31], v2 offset:16384\n"
    "ds_read_b64_tr_b16 v[164:165], v4 offset:8192\n"
    "ds_read_b64_tr_b16 v[166:167], v4 offset:8704\n"
    "v_mfma_f32_16x16x32_bf16 v[236:239], v[156:159], v[12:15], v[236:239]\n"
    "ds_read_b128 v[12:15], v2 offset:18432\n"
    "v_mfma_f32_16x16x32_bf16 v[240:243], v[152:155], v[16:19], v[240:243]\n"
    "v_mfma_f32_16x16x32_bf16 v[244:247], v[156:159], v[16:19], v[244:247]\n"
    "ds_read_b128 v[16:19], v2 offset:20480\n"
    "v_mfma_f32_16x16x32_bf16 v[248:251], v[152:155], v[20:23], v[248:251]\n"
    "v_mfma_f32_16x16x32_bf16 v[252:255], v[156:159], v[20:23], v[252:255]\n"
    "ds_read_b128 v[20:23], v2 offset:22528\n"
    "v_mfma_f32_16x16x32_bf16 v[184:187], v[152:155], v[24:27], v[184:187]\n"
    "v_mfma_f32_16x16x32_bf16 v[188:191], v[156:159], v[24:27], v[188:191]\n"
    "ds_read_b128 v[24:27], v2 offset:24576\n"
    "s_waitcnt lgkmcnt(4)\n"
    "v_mfma_f32_16x16x32_bf16 v[192:195], v[160:163], v[28:31], v[192:195]\n"
    "v_mfma_f32_16x16x32_bf16 v[196:199], v[164:167], v[28:31], v[196:199]\n"
    "ds_read_b128 v[28:31], v2 offset:26624\n"
    "s_waitcnt lgkmcnt(4)\n"
    "v_mfma_f32_16x16x32_bf16 v[200:203], v[160:163], v[12:15], v[200:203]\n"
    "v_mfma_f32_16x16x32_bf16 v[204:207], v[164:167], v[12:15], v[204:207]\n"
    "ds_read_b128 v[12:15], v2 offset:28672\n"
    "s_waitcnt lgkmcnt(4)\n"
    "v_mfma_f32_16x16x32_bf16 v[208:211], v[160:163], v[16:19], v[208:211]\n"
    "v_mfma_f32_16x16x32_bf16 v[212:215], v[164:167], v[16:19], v[212:215]\n"
    "ds_read_b128 v[16:19], v2 offset:30720\n"
    "s_waitcnt lgkmcnt(4)\n"
    "v_mfma_f32_16x16x32_bf16 v[216:219], v[160:163], v[20:23], v[216:219]\n"
    "v_mfma_f32_16x16x32_bf16 v[220:223], v[164:167], v[20:23], v[220:223]\n"
    "ds_read_b128 v[20:23], v2 offset:32768\n"
    "s_waitcnt lgkmcnt(4)\n"
    "v_mfma_f32_16x16x32_bf16 v[224:227], v[160:163], v[24:27], v[224:227]\n"
    "ds_read_b64_tr_b16 v[168:169], v4 offset:4096\n"
    "v_mfma_f32_16x16x32_bf16 v[228:231], v[164:167], v[24:27], v[228:231]\n"
    "ds_read_b128 v[24:27], v3 offset:16384\n"
    "s_waitcnt lgkmcnt(5)\n"
    "v_mfma_f32_16x16x32_bf16 v[232:235], v[160:163], v[28:31], v[232:235]\n"
    "ds_read_b64_tr_b16 v[170:171], v4 offset:4608\n"
    "v_mfma_f32_16x16x32_bf16 v[236:239], v[164:167], v[28:31], v[236:239]\n"
    "ds_read_b128 v[28:31], v3 offset:18432\n"
    "s_waitcnt lgkmcnt(6)\n"
    "v_mfma_f32_16x16x32_bf16 v[240:243], v[160:163], v[12:15], v[240:243]\n"
    "ds_read_b64_tr_b16 v[172:173], v4 offset:12288\n"
    "v_mfma_f32_16x16x32_bf16 v[244:247], v[164:167], v[12:15], v[244:247]\n"
    "ds_read_b128 v[12:15], v3 offset:20480\n"
    "s_waitcnt lgkmcnt(7)\n"
    "v_mfma_f32_16x16x32_bf16 v[248:251], v[160:163], v[16:19], v[248:251]\n"
    "ds_read_b64_tr_b16 v[174:175], v4 offset:12800\n"
    "v_mfma_f32_16x16x32_bf16 v[252:255], v[164:167], v[16:19], v[252:255]\n"
    "ds_read_b128 v[16:19], v3 offset:22528\n"
    "s_waitcnt lgkmcnt(8)\n"
    "v_mfma_f32_16x16x32_bf16 v[184:187], v[160:163], v[20:23], v[184:187]\n"
    "v_mfma_f32_16x16x32_bf16 v[188:191], v[164:167], v[20:23], v[188:191]\n"
    "ds_read_b128 v[20:23], v3 offset:24576\n"
    "s_waitcnt lgkmcnt(2)\n"
    "v_mfma_f32_16x16x32_bf16 v[192:195], v[168:171], v[24:27], v[192:195]\n"
    "v_mfma_f32_16x16x32_bf16 v[196:199], v[172:175], v[24:27], v[196:199]\n"
    "ds_read_b128 v[24:27], v3 offset:26624\n"
    "v_mfma_f32_16x16x32_bf16 v[200:203], v[168:171], v[28:31], v[200:203]\n"
    "v_mfma_f32_16x16x32_bf16 v[204:207], v[172:175], v[28:31], v[204:207]\n"
    "ds_read_b128 v[28:31], v3 offset:28672\n"
    "v_mfma_f32_16x16x32_bf16 v[208:211], v[168:171], v[12:15], v[208:211]\n"
    "v_mfma_f32_16x16x32_bf16 v[212:215], v[172:175], v[12:15], v[212:215]\n"
    "ds_read_b128 v[12:15], v3 offset:30720\n"
    "s_waitcnt lgkmcnt(4)\n"
    "v_mfma_f32_16x16x32_bf16 v[216:219], v[168:171], v[16:19], v[216:219]\n"
    "v_mfma_f32_16x16x32_bf16 v[220:223], v[172:175], v[16:19], v[220:223]\n"
    "ds_read_b128 v[16:19], v3 offset:32768\n"
    "s_waitcnt lgkmcnt(4)\n"
    "v_mfma_f32_16x16x32_bf16 v[224:227], v[168:171], v[20:23], v[224:227]\n"
    "v_mfma_f32_16x16x32_bf16 v[228:231], v[172:175], v[20:23], v[228:231]\n"
    "s_waitcnt lgkmcnt(3)\n"
    "v_mfma_f32_16x16x32_bf16 v[232:235], v[168:171], v[24:27], v[232:235]\n"
    "s_waitcnt lgkmcnt(0)\n"
    "s_barrier\n"
    "v_add_u32 v4, 34816, v4\n"
    "ds_read_b64_tr_b16 v[176:177], v4 offset:0\n"
    "ds_read_b64_tr_b16 v[178:179], v4 offset:512\n"
    "v_add_u32 v2, 34816, v2\n"
    "v_add_u32 v3, 34816, v3\n"
    "ds_read_b128 v[20:23], v2 offset:16384\n"
    "ds_read_b64_tr_b16 v[180:181], v4 offset:8192\n"
    "ds_read_b64_tr_b16 v[182:183], v4 offset:8704\n"
    "v_mfma_f32_16x16x32_bf16 v[236:239], v[172:175], v[24:27], v[236:239]\n"
    "ds_read_b128 v[24:27], v2 offset:18432\n"
    "v_mfma_f32_16x16x32_bf16 v[240:243], v[168:171], v[28:31], v[240:243]\n"
    "v_mfma_f32_16x16x32_bf16 v[244:247], v[172:175], v[28:31], v[244:247]\n"
    "ds_read_b128 v[28:31], v2 offset:20480\n"
    "v_mfma_f32_16x16x32_bf16 v[248:251], v[168:171], v[12:15], v[248:251]\n"
    "v_mfma_f32_16x16x32_bf16 v[252:255], v[172:175], v[12:15], v[252:255]\n"
    "ds_read_b128 v[12:15], v2 offset:22528\n"
    "v_mfma_f32_16x16x32_bf16 v[184:187], v[168:171], v[16:19], v[184:187]\n"
    "v_mfma_f32_16x16x32_bf16 v[188:191], v[172:175], v[16:19], v[188:191]\n"
    "ds_read_b128 v[16:19], v2 offset:24576\n"
    "s_waitcnt lgkmcnt(4)\n"
    "v_mfma_f32_16x16x32_bf16 v[192:195], v[176:179], v[20:23], v[192:195]\n"
    "v_mfma_f32_16x16x32_bf16 v[196:199], v[180:183], v[20:23], v[196:199]\n"
    "ds_read_b128 v[20:23], v2 offset:26624\n"
    "s_waitcnt lgkmcnt(4)\n"
    "v_mfma_f32_16x16x32_bf16 v[200:203], v[176:179], v[24:27], v[200:203]\n"
    "v_mfma_f32_16x16x32_bf16 v[204:207], v[180:183], v[24:27], v[204:207]\n"
    "ds_read_b128 v[24:27], v2 offset:28672\n"
    "s_waitcnt lgkmcnt(4)\n"
    "v_mfma_f32_16x16x32_bf16 v[208:211], v[176:179], v[28:31], v[208:211]\n"
    "v_mfma_f32_16x16x32_bf16 v[212:215], v[180:183], v[28:31], v[212:215]\n"
    "ds_read_b128 v[28:31], v2 offset:30720\n"
    "s_waitcnt lgkmcnt(4)\n"
    "v_mfma_f32_16x16x32_bf16 v[216:219], v[176:179], v[12:15], v[216:219]\n"
    "v_mfma_f32_16x16x32_bf16 v[220:223], v[180:183], v[12:15], v[220:223]\n"
    "ds_read_b128 v[12:15], v2 offset:32768\n"
    "s_waitcnt lgkmcnt(4)\n"
    "v_mfma_f32_16x16x32_bf16 v[224:227], v[176:179], v[16:19], v[224:227]\n"
    "ds_read_b64_tr_b16 v[32:33], v4 offset:4096\n"
    "v_mfma_f32_16x16x32_bf16 v[228:231], v[180:183], v[16:19], v[228:231]\n"
    "ds_read_b128 v[16:19], v3 offset:32768\n"
    "s_waitcnt lgkmcnt(5)\n"
    "v_mfma_f32_16x16x32_bf16 v[232:235], v[176:179], v[20:23], v[232:235]\n"
    "ds_read_b64_tr_b16 v[34:35], v4 offset:4608\n"
    "v_mfma_f32_16x16x32_bf16 v[236:239], v[180:183], v[20:23], v[236:239]\n"
    "ds_read_b128 v[20:23], v3 offset:16384\n"
    "s_waitcnt lgkmcnt(6)\n"
    "v_mfma_f32_16x16x32_bf16 v[240:243], v[176:179], v[24:27], v[240:243]\n"
    "ds_read_b64_tr_b16 v[36:37], v4 offset:12288\n"
    "v_mfma_f32_16x16x32_bf16 v[244:247], v[180:183], v[24:27], v[244:247]\n"
    "ds_read_b128 v[24:27], v3 offset:18432\n"
    "s_waitcnt lgkmcnt(7)\n"
    "v_mfma_f32_16x16x32_bf16 v[248:251], v[176:179], v[28:31], v[248:251]\n"
    "ds_read_b64_tr_b16 v[38:39], v4 offset:12800\n"
    "v_mfma_f32_16x16x32_bf16 v[252:255], v[180:183], v[28:31], v[252:255]\n"
    "ds_read_b128 v[28:31], v3 offset:20480\n"
    "s_waitcnt lgkmcnt(8)\n"
    "v_mfma_f32_16x16x32_bf16 v[184:187], v[176:179], v[12:15], v[184:187]\n"
    "v_mfma_f32_16x16x32_bf16 v[188:191], v[180:183], v[12:15], v[188:191]\n"
    "ds_read_b128 v[12:15], v3 offset:22528\n"
    "s_waitcnt lgkmcnt(2)\n"
    "v_mfma_f32_16x16x32_bf16 v[184:187], v[32:35], v[16:19], v[184:187]\n"
    "v_mfma_f32_16x16x32_bf16 v[188:191], v[36:39], v[16:19], v[188:191]\n"
    "ds_read_b128 v[16:19], v3 offset:24576\n"
    "v_mfma_f32_16x16x32_bf16 v[192:195], v[32:35], v[20:23], v[192:195]\n"
    "v_mfma_f32_16x16x32_bf16 v[196:199], v[36:39], v[20:23], v[196:199]\n"
    "ds_read_b128 v[20:23], v3 offset:26624\n"
    "v_mfma_f32_16x16x32_bf16 v[200:203], v[32:35], v[24:27], v[200:203]\n"
    "v_mfma_f32_16x16x32_bf16 v[204:207], v[36:39], v[24:27], v[204:207]\n"
    "ds_read_b128 v[24:27], v3 offset:28672\n"
    "s_waitcnt lgkmcnt(4)\n"
    "v_mfma_f32_16x16x32_bf16 v[208:211], v[32:35], v[28:31], v[208:211]\n"
    "v_mul_f32 v184, s52, v184\n"
    "v_mul_f32 v185, s52, v185\n"
    "v_mfma_f32_16x16x32_bf16 v[212:215], v[36:39], v[28:31], v[212:215]\n"
    "ds_read_b128 v[28:31], v3 offset:30720\n"
    "v_mul_f32 v186, s52, v186\n"
    "v_mul_f32 v187, s52, v187\n"
    "s_waitcnt lgkmcnt(4)\n"
    "v_mfma_f32_16x16x32_bf16 v[216:219], v[32:35], v[12:15], v[216:219]\n"
    "v_exp_f32 v184, v184\n"
    "v_exp_f32 v185, v185\n"
    "v_mfma_f32_16x16x32_bf16 v[220:223], v[36:39], v[12:15], v[220:223]\n"
    "v_exp_f32 v186, v186\n"
    "v_exp_f32 v187, v187\n"
    "s_waitcnt lgkmcnt(3)\n"
    "v_mfma_f32_16x16x32_bf16 v[224:227], v[32:35], v[16:19], v[224:227]\n"
    "s_nop 0\n"
    "v_add_f32 v184, 1.0, v184\n"
    "v_mfma_f32_16x16x32_bf16 v[228:231], v[36:39], v[16:19], v[228:231]\n"
    "v_add_f32 v185, 1.0, v185\n"
    "v_add_f32 v186, 1.0, v186\n"
    "s_waitcnt lgkmcnt(2)\n"
    "v_mfma_f32_16x16x32_bf16 v[232:235], v[32:35], v[20:23], v[232:235]\n"
    "v_add_f32 v187, 1.0, v187\n"
    "v_rcp_f32 v184, v184\n"
    "v_mfma_f32_16x16x32_bf16 v[236:239], v[36:39], v[20:23], v[236:239]\n"
    "v_rcp_f32 v185, v185\n"
    "v_rcp_f32 v186, v186\n"
    "s_waitcnt lgkmcnt(1)\n"
    "v_mfma_f32_16x16x32_bf16 v[240:243], v[32:35], v[24:27], v[240:243]\n"
    "v_rcp_f32 v187, v187\n"
    "s_nop 0\n"
    "v_mfma_f32_16x16x32_bf16 v[244:247], v[36:39], v[24:27], v[244:247]\n"
    "v_mul_f32 v184, v184, v188\n"
    "v_mul_f32 v185, v185, v189\n"
    "s_waitcnt lgkmcnt(0)\n"
    "v_mfma_f32_16x16x32_bf16 v[248:251], v[32:35], v[28:31], v[248:251]\n"
    "v_mul_f32 v186, v186, v190\n"
    "v_mul_f32 v187, v187, v191\n"
    "v_mfma_f32_16x16x32_bf16 v[252:255], v[36:39], v[28:31], v[252:255]\n"
    "v_cvt_pk_bf16_f32 v188, v184, v185\n"
    "v_cvt_pk_bf16_f32 v189, v186, v187\n"
    "s_nop 1\n"
    "v_permlane16_swap_b32 v188, v190\n"
    "v_permlane16_swap_b32 v189, v191\n"
    "buffer_store_dwordx4 v[188:191], v8, s[28:31], s91 offen sc1\n"
    "v_pk_mul_f32 v[192:193], v[192:193], s[52:53]\n"
    "v_pk_mul_f32 v[194:195], v[194:195], s[52:53]\n"
    "v_pk_mul_f32 v[200:201], v[200:201], s[52:53]\n"
    "v_pk_mul_f32 v[202:203], v[202:203], s[52:53]\n"
    "v_exp_f32 v192, v192\n"
    "v_exp_f32 v193, v193\n"
    "v_exp_f32 v194, v194\n"
    "v_exp_f32 v195, v195\n"
    "v_exp_f32 v200, v200\n"
    "v_exp_f32 v201, v201\n"
    "v_exp_f32 v202, v202\n"
    "v_exp_f32 v203, v203\n"
    "v_pk_add_f32 v[192:193], v[192:193], 1.0 op_sel_hi:[1,0]\n"
    "v_pk_add_f32 v[194:195], v[194:195], 1.0 op_sel_hi:[1,0]\n"
    "v_pk_add_f32 v[200:201], v[200:201], 1.0 op_sel_hi:[1,0]\n"
    "v_pk_add_f32 v[202:203], v[202:203], 1.0 op_sel_hi:[1,0]\n"
    "s_lshl_b32 s48, s3, 11\n"
    "s_add_u32 s48, s48, 104448\n"
    "v_add_u32 v9, s48, v7\n"
    "ds_read_b128 v[184:187], v9\n"
    "ds_read_b128 v[188:191], v9 offset:1024\n"
    "v_rcp_f32 v192, v192\n"
    "v_rcp_f32 v193, v193\n"
    "v_rcp_f32 v194, v194\n"
    "v_rcp_f32 v195, v195\n"
    "v_rcp_f32 v200, v200\n"
    "v_rcp_f32 v201, v201\n"
    "v_rcp_f32 v202, v202\n"
    "v_rcp_f32 v203, v203\n"
    "v_pk_mul_f32 v[192:193], v[192:193], v[196:197]\n"
    "v_pk_mul_f32 v[194:195], v[194:195], v[198:199]\n"
    "v_pk_mul_f32 v[200:201], v[200:201], v[204:205]\n"
    "v_pk_mul_f32 v[202:203], v[202:203], v[206:207]\n"
    "v_cvt_pk_bf16_f32 v196, v192, v193\n"
    "v_cvt_pk_bf16_f32 v197, v194, v195\n"
    "v_cvt_pk_bf16_f32 v198, v200, v201\n"
    "v_cvt_pk_bf16_f32 v199, v202, v203\n"
    "s_nop 1\n"
    "v_permlane16_swap_b32 v196, v198\n"
    "v_permlane16_swap_b32 v197, v199\n"
    "buffer_store_dwordx4 v[196:199], v0, s[28:31], s90 offen sc1\n"
    "s_nop 1\n"
    "s_add_u32 s91, s90, 98304\n"
    "v_pk_mul_f32 v[208:209], v[208:209], s[52:53]\n"
    "v_pk_mul_f32 v[210:211], v[210:211], s[52:53]\n"
    "v_pk_mul_f32 v[216:217], v[216:217], s[52:53]\n"
    "v_pk_mul_f32 v[218:219], v[218:219], s[52:53]\n"
    "v_exp_f32 v208, v208\n"
    "v_exp_f32 v209, v209\n"
    "v_exp_f32 v210, v210\n"
    "v_exp_f32 v211, v211\n"
    "v_exp_f32 v216, v216\n"
    "v_exp_f32 v217, v217\n"
    "v_exp_f32 v218, v218\n"
    "v_exp_f32 v219, v219\n"
    "v_pk_add_f32 v[208:209], v[208:209], 1.0 op_sel_hi:[1,0]\n"
    "v_pk_add_f32 v[210:211], v[210:211], 1.0 op_sel_hi:[1,0]\n"
    "v_pk_add_f32 v[216:217], v[216:217], 1.0 op_sel_hi:[1,0]\n"
    "v_pk_add_f32 v[218:219], v[218:219], 1.0 op_sel_hi:[1,0]\n"
    "v_rcp_f32 v208, v208\n"
    "v_rcp_f32 v209, v209\n"
    "v_rcp_f32 v210, v210\n"
    "v_rcp_f32 v211, v211\n"
    "v_rcp_f32 v216, v216\n"
    "v_rcp_f32 v217, v217\n"
    "s_barrier\n"
    "v_add_u32 v2, 53248, v2\n"
    "v_add_u32 v3, 53248, v3\n"
    "ds_read_b128 v[12:15], v2 offset:0\n"
    "ds_read_b128 v[16:19], v2 offset:2048\n"
    "ds_read_b128 v[20:23], v3 offset:0\n"
    "ds_read_b128 v[24:27], v3 offset:2048\n"
    "ds_read_b128 v[28:31], v2 offset:4096\n"
    "v_rcp_f32 v218, v218\n"
    "v_rcp_f32 v219, v219\n"
    "v_pk_mul_f32 v[208:209], v[208:209], v[212:213]\n"
    "v_pk_mul_f32 v[210:211], v[210:211], v[214:215]\n"
    "v_pk_mul_f32 v[216:217], v[216:217], v[220:221]\n"
    "v_pk_mul_f32 v[218:219], v[218:219], v[222:223]\n"
    "v_cvt_pk_bf16_f32 v212, v208, v209\n"
    "v_cvt_pk_bf16_f32 v213, v210, v211\n"
    "v_cvt_pk_bf16_f32 v214, v216, v217\n"
    "v_cvt_pk_bf16_f32 v215, v218, v219\n"
    "s_nop 1\n"
    "v_permlane16_swap_b32 v212, v214\n"
    "v_permlane16_swap_b32 v213, v215\n"
    "buffer_store_dwordx4 v[212:215], v0, s[28:31], s91 offen sc1\n"
    "s_waitcnt lgkmcnt(3)\n"
    "v_mfma_f32_16x16x32_bf16 v[192:195], v[184:187], v[12:15], 0\n"
    "v_mfma_f32_16x16x32_bf16 v[196:199], v[188:191], v[12:15], 0\n"
    "ds_read_b128 v[12:15], v2 offset:6144\n"
    "v_mfma_f32_16x16x32_bf16 v[200:203], v[184:187], v[16:19], 0\n"
    "s_add_u32 s91, s90, 196608\n"
    "v_mul_f32 v224, s52, v224\n"
    "v_mfma_f32_16x16x32_bf16 v[204:207], v[188:191], v[16:19], 0\n"
    "ds_read_b128 v[16:19], v3 offset:4096\n"
    "v_mul_f32 v225, s52, v225\n"
    "s_waitcnt lgkmcnt(3)\n"
    "v_mfma_f32_16x16x32_bf16 v[192:195], v[40:43], v[20:23], v[192:195]\n"
    "v_mul_f32 v226, s52, v226\n"
    "v_mfma_f32_16x16x32_bf16 v[196:199], v[44:47], v[20:23], v[196:199]\n"
    "ds_read_b128 v[20:23], v3 offset:6144\n"
    "v_mul_f32 v227, s52, v227\n"
    "v_mfma_f32_16x16x32_bf16 v[200:203], v[40:43], v[24:27], v[200:203]\n"
    "v_mul_f32 v232, s52, v232\n"
    "v_mul_f32 v233, s52, v233\n"
    "v_mfma_f32_16x16x32_bf16 v[204:207], v[44:47], v[24:27], v[204:207]\n"
    "ds_read_b128 v[24:27], v2 offset:8192\n"
    "v_mul_f32 v234, s52, v234\n"
    "s_waitcnt lgkmcnt(3)\n"
    "v_mfma_f32_16x16x32_bf16 v[192:195], v[48:51], v[28:31], v[192:195]\n"
    "v_mul_f32 v235, s52, v235\n"
    "v_mfma_f32_16x16x32_bf16 v[196:199], v[52:55], v[28:31], v[196:199]\n"
    "ds_read_b128 v[28:31], v2 offset:10240\n"
    "v_exp_f32 v224, v224\n"
    "v_mfma_f32_16x16x32_bf16 v[200:203], v[48:51], v[12:15], v[200:203]\n"
    "v_exp_f32 v225, v225\n"
    "v_mfma_f32_16x16x32_bf16 v[204:207], v[52:55], v[12:15], v[204:207]\n"
    "ds_read_b128 v[12:15], v3 offset:8192\n"
    "s_waitcnt lgkmcnt(3)\n"
    "v_mfma_f32_16x16x32_bf16 v[192:195], v[56:59], v[16:19], v[192:195]\n"
    "v_exp_f32 v226, v226\n"
    "v_mfma_f32_16x16x32_bf16 v[196:199], v[60:63], v[16:19], v[196:199]\n"
    "ds_read_b128 v[16:19], v3 offset:10240\n"
    "v_mfma_f32_16x16x32_bf16 v[200:203], v[56:59], v[20:23], v[200:203]\n"
    "v_exp_f32 v227, v227\n"
    "v_mfma_f32_16x16x32_bf16 v[204:207], v[60:63], v[20:23], v[204:207]\n"
    "ds_read_b128 v[20:23], v2 offset:12288\n"
    "s_waitcnt lgkmcnt(3)\n"
    "v_mfma_f32_16x16x32_bf16 v[192:195], v[64:67], v[24:27], v[192:195]\n"
    "v_exp_f32 v232, v232\n"
    "v_mfma_f32_16x16x32_bf16 v[196:199], v[68:71], v[24:27], v[196:199]\n"
    "ds_read_b128 v[24:27], v2 offset:14336\n"
    "v_exp_f32 v233, v233\n"
    "v_mfma_f32_16x16x32_bf16 v[200:203], v[64:67], v[28:31], v[200:203]\n"
    "v_exp_f32 v234, v234\n"
    "v_mfma_f32_16x16x32_bf16 v[204:207], v[68:71], v[28:31], v[204:207]\n"
    "ds_read_b128 v[28:31], v3 offset:12288\n"
    "s_waitcnt lgkmcnt(3)\n"
    "v_mfma_f32_16x16x32_bf16 v[192:195], v[72:75], v[12:15], v[192:195]\n"
    "v_exp_f32 v235, v235\n"
    "v_mfma_f32_16x16x32_bf16 v[196:199], v[76:79], v[12:15], v[196:199]\n"
    "ds_read_b128 v[12:15], v3 offset:14336\n"
    "v_add_f32 v224, 1.0, v224\n"
    "v_mfma_f32_16x16x32_bf16 v[200:203], v[72:75], v[16:19], v[200:203]\n"
    "v_add_f32 v225, 1.0, v225\n"
    "v_mfma_f32_16x16x32_bf16 v[204:207], v[76:79], v[16:19], v[204:207]\n"
    "ds_read_b128 v[16:19], v2 offset:16384\n"
    "v_add_f32 v226, 1.0, v226\n"
    "s_waitcnt lgkmcnt(3)\n"
    "v_mfma_f32_16x16x32_bf16 v[192:195], v[80:83], v[20:23], v[192:195]\n"
    "v_add_f32 v227, 1.0, v227\n"
    "v_add_f32 v232, 1.0, v232\n"
    "v_mfma_f32_16x16x32_bf16 v[196:199], v[84:87], v[20:23], v[196:199]\n"
    "ds_read_b128 v[20:23], v2 offset:18432\n"
    "v_add_f32 v233, 1.0, v233\n"
    "v_mfma_f32_16x16x32_bf16 v[200:203], v[80:83], v[24:27], v[200:203]\n"
    "v_add_f32 v234, 1.0, v234\n"
    "v_add_f32 v235, 1.0, v235\n"
    "v_mfma_f32_16x16x32_bf16 v[204:207], v[84:87], v[24:27], v[204:207]\n"
    "ds_read_b128 v[24:27], v3 offset:16384\n"
    "s_waitcnt lgkmcnt(3)\n"
    "v_mfma_f32_16x16x32_bf16 v[192:195], v[88:91], v[28:31], v[192:195]\n"
    "v_rcp_f32 v224, v224\n"
    "v_mfma_f32_16x16x32_bf16 v[196:199], v[92:95], v[28:31], v[196:199]\n"
    "ds_read_b128 v[28:31], v3 offset:18432\n"
    "v_mfma_f32_16x16x32_bf16 v[200:203], v[88:91], v[12:15], v[200:203]\n"
    "v_rcp_f32 v225, v225\n"
    "v_mfma_f32_16x16x32_bf16 v[204:207], v[92:95], v[12:15], v[204:207]\n"
    "ds_read_b128 v[12:15], v2 offset:20480\n"
    "v_rcp_f32 v226, v226\n"
    "s_waitcnt lgkmcnt(3)\n"
    "v_mfma_f32_16x16x32_bf16 v[192:195], v[96:99], v[16:19], v[192:195]\n"
    "v_rcp_f32 v227, v227\n"
    "v_mfma_f32_16x16x32_bf16 v[196:199], v[100:103], v[16:19], v[196:199]\n"
    "ds_read_b128 v[16:19], v2 offset:22528\n"
    "v_mfma_f32_16x16x32_bf16 v[200:203], v[96:99], v[20:23], v[200:203]\n"
    "v_rcp_f32 v232, v232\n"
    "v_mfma_f32_16x16x32_bf16 v[204:207], v[100:103], v[20:23], v[204:207]\n"
    "ds_read_b128 v[20:23], v3 offset:20480\n"
    "s_waitcnt lgkmcnt(3)\n"
    "v_mfma_f32_16x16x32_bf16 v[192:195], v[104:107], v[24:27], v[192:195]\n"
    "v_rcp_f32 v233, v233\n"
    "v_mfma_f32_16x16x32_bf16 v[196:199], v[108:111], v[24:27], v[196:199]\n"
    "ds_read_b128 v[24:27], v3 offset:22528\n"
    "v_rcp_f32 v234, v234\n"
    "v_mfma_f32_16x16x32_bf16 v[200:203], v[104:107], v[28:31], v[200:203]\n"
    "v_mfma_f32_16x16x32_bf16 v[204:207], v[108:111], v[28:31], v[204:207]\n"
    "ds_read_b128 v[28:31], v2 offset:24576\n"
    "v_rcp_f32 v235, v235\n"
    "s_waitcnt lgkmcnt(3)\n"
    "v_mfma_f32_16x16x32_bf16 v[192:195], v[112:115], v[12:15], v[192:195]\n"
    "v_mul_f32 v224, v224, v228\n"
    "v_mul_f32 v225, v225, v229\n"
    "v_mfma_f32_16x16x32_bf16 v[196:199], v[116:119], v[12:15], v[196:199]\n"
    "ds_read_b128 v[12:15], v2 offset:26624\n"
    "v_mfma_f32_16x16x32_bf16 v[200:203], v[112:115], v[16:19], v[200:203]\n"
    "v_mul_f32 v226, v226, v230\n"
    "v_mul_f32 v227, v227, v231\n"
    "v_mfma_f32_16x16x32_bf16 v[204:207], v[116:119], v[16:19], v[204:207]\n"
    "ds_read_b128 v[16:19], v3 offset:24576\n"
    "v_mul_f32 v232, v232, v236\n"
    "s_waitcnt lgkmcnt(3)\n"
    "v_mfma_f32_16x16x32_bf16 v[192:195], v[120:123], v[20:23], v[192:195]\n"
    "v_mul_f32 v233, v233, v237\n"
    "v_mul_f32 v234, v234, v238\n"
    "v_mfma_f32_16x16x32_bf16 v[196:199], v[124:127], v[20:23], v[196:199]\n"
    "ds_read_b128 v[20:23], v3 offset:26624\n"
    "v_mfma_f32_16x16x32_bf16 v[200:203], v[120:123], v[24:27], v[200:203]\n"
    "v_mul_f32 v235, v235, v239\n"
    "v_cvt_pk_bf16_f32 v228, v224, v225\n"
    "v_mfma_f32_16x16x32_bf16 v[204:207], v[124:127], v[24:27], v[204:207]\n"
    "ds_read_b128 v[24:27], v2 offset:28672\n"
    "v_cvt_pk_bf16_f32 v229, v226, v227\n"
    "s_waitcnt lgkmcnt(3)\n"
    "v_mfma_f32_16x16x32_bf16 v[192:195], v[128:131], v[28:31], v[192:195]\n"
    "v_cvt_pk_bf16_f32 v230, v232, v233\n"
    "v_cvt_pk_bf16_f32 v231, v234, v235\n"
    "v_mfma_f32_16x16x32_bf16 v[196:199], v[132:135], v[28:31], v[196:199]\n"
    "ds_read_b128 v[28:31], v2 offset:30720\n"
    "s_nop 1\n"
    "v_mfma_f32_16x16x32_bf16 v[200:203], v[128:131], v[12:15], v[200:203]\n"
    "v_permlane16_swap_b32 v228, v230\n"
    "v_mfma_f32_16x16x32_bf16 v[204:207], v[132:135], v[12:15], v[204:207]\n"
    "ds_read_b128 v[12:15], v3 offset:28672\n"
    "v_permlane16_swap_b32 v229, v231\n"
    "s_waitcnt lgkmcnt(3)\n"
    "v_mfma_f32_16x16x32_bf16 v[192:195], v[136:139], v[16:19], v[192:195]\n"
    "buffer_store_dwordx4 v[228:231], v0, s[28:31], s91 offen sc1\n"
    "s_add_u32 s91, s90, 294912\n"
    "v_mfma_f32_16x16x32_bf16 v[196:199], v[140:143], v[16:19], v[196:199]\n"
    "ds_read_b128 v[16:19], v3 offset:30720\n"
    "v_mul_f32 v240, s52, v240\n"
    "v_mfma_f32_16x16x32_bf16 v[200:203], v[136:139], v[20:23], v[200:203]\n"
    "v_mul_f32 v241, s52, v241\n"
    "v_mul_f32 v242, s52, v242\n"
    "v_mfma_f32_16x16x32_bf16 v[204:207], v[140:143], v[20:23], v[204:207]\n"
    "ds_read_b128 v[20:23], v2 offset:32768\n"
    "s_waitcnt lgkmcnt(3)\n"
    "v_mfma_f32_16x16x32_bf16 v[192:195], v[144:147], v[24:27], v[192:195]\n"
    "v_mul_f32 v243, s52, v243\n"
    "v_mul_f32 v248, s52, v248\n"
    "v_mfma_f32_16x16x32_bf16 v[196:199], v[148:151], v[24:27], v[196:199]\n"
    "ds_read_b128 v[24:27], v2 offset:34816\n"
    "v_mul_f32 v249, s52, v249\n"
    "v_mfma_f32_16x16x32_bf16 v[200:203], v[144:147], v[28:31], v[200:203]\n"
    "v_mul_f32 v250, s52, v250\n"
    "v_mul_f32 v251, s52, v251\n"
    "v_mfma_f32_16x16x32_bf16 v[204:207], v[148:151], v[28:31], v[204:207]\n"
    "ds_read_b128 v[28:31], v3 offset:32768\n"
    "s_waitcnt lgkmcnt(3)\n"
    "v_mfma_f32_16x16x32_bf16 v[192:195], v[152:155], v[12:15], v[192:195]\n"
    "v_exp_f32 v240, v240\n"
    "v_mfma_f32_16x16x32_bf16 v[196:199], v[156:159], v[12:15], v[196:199]\n"
    "ds_read_b128 v[12:15], v3 offset:34816\n"
    "v_mfma_f32_16x16x32_bf16 v[200:203], v[152:155], v[16:19], v[200:203]\n"
    "v_exp_f32 v241, v241\n"
    "v_mfma_f32_16x16x32_bf16 v[204:207], v[156:159], v[16:19], v[204:207]\n"
    "ds_read_b128 v[16:19], v2 offset:36864\n"
    "v_exp_f32 v242, v242\n"
    "s_waitcnt lgkmcnt(3)\n"
    "v_mfma_f32_16x16x32_bf16 v[192:195], v[160:163], v[20:23], v[192:195]\n"
    "v_exp_f32 v243, v243\n"
    "v_mfma_f32_16x16x32_bf16 v[196:199], v[164:167], v[20:23], v[196:199]\n"
    "ds_read_b128 v[20:23], v2 offset:38912\n"
    "v_mfma_f32_16x16x32_bf16 v[200:203], v[160:163], v[24:27], v[200:203]\n"
    "v_exp_f32 v248, v248\n"
    "v_mfma_f32_16x16x32_bf16 v[204:207], v[164:167], v[24:27], v[204:207]\n"
    "ds_read_b128 v[24:27], v3 offset:36864\n"
    "s_waitcnt lgkmcnt(3)\n"
    "v_mfma_f32_16x16x32_bf16 v[192:195], v[168:171], v[28:31], v[192:195]\n"
    "v_exp_f32 v249, v249\n"
    "v_mfma_f32_16x16x32_bf16 v[196:199], v[172:175], v[28:31], v[196:199]\n"
    "ds_read_b128 v[28:31], v3 offset:38912\n"
    "v_exp_f32 v250, v250\n"
    "v_mfma_f32_16x16x32_bf16 v[200:203], v[168:171], v[12:15], v[200:203]\n"
    "s_branch PAD2_%=\n"
    ".p2align 12\n"
    "PAD2_%=:\n"
    "s_cbranch_execz PADRET_%=\n"
    "s_barrier\n"
    "v_exp_f32 v251, v251\n"
    "v_mfma_f32_16x16x32_bf16 v[204:207], v[172:175], v[12:15], v[204:207]\n"
    "v_subrev_u32 v2, 122880, v2\n"
    "v_subrev_u32 v3, 122880, v3\n"
    "ds_read_b128 v[12:15], v2 offset:0\n"
    "s_waitcnt lgkmcnt(3)\n"
    "v_mfma_f32_16x16x32_bf16 v[192:195], v[176:179], v[16:19], v[192:195]\n"
    "v_add_f32 v240, 1.0, v240\n"
    "v_add_f32 v241, 1.0, v241\n"
    "v_mfma_f32_16x16x32_bf16 v[196:199], v[180:183], v[16:19], v[196:199]\n"
    "ds_read_b128 v[16:19], v2 offset:2048\n"
    "v_mfma_f32_16x16x32_bf16 v[200:203], v[176:179], v[20:23], v[200:203]\n"
    "v_add_f32 v242, 1.0, v242\n"
    "v_add_f32 v243, 1.0, v243\n"
    "v_mfma_f32_16x16x32_bf16 v[204:207], v[180:183], v[20:23], v[204:207]\n"
    "ds_read_b128 v[20:23], v3 offset:0\n"
    "v_add_f32 v248, 1.0, v248\n"
    "s_waitcnt lgkmcnt(3)\n"
    "v_mfma_f32_16x16x32_bf16 v[192:195], v[32:35], v[24:27], v[192:195]\n"
    "v_add_f32 v249, 1.0, v249\n"
    "v_add_f32 v250, 1.0, v250\n"
    "v_mfma_f32_16x16x32_bf16 v[196:199], v[36:39], v[24:27], v[196:199]\n"
    "ds_read_b128 v[24:27], v3 offset:2048\n"
    "v_add_f32 v251, 1.0, v251\n"
    "v_mfma_f32_16x16x32_bf16 v[200:203], v[32:35], v[28:31], v[200:203]\n"
    "v_rcp_f32 v240, v240\n"
    "v_mfma_f32_16x16x32_bf16 v[204:207], v[36:39], v[28:31], v[204:207]\n"
    "ds_read_b128 v[28:31], v2 offset:4096\n"
    "s_waitcnt lgkmcnt(3)\n"
    "v_mfma_f32_16x16x32_bf16 v[208:211], v[184:187], v[12:15], 0\n"
    "v_rcp_f32 v241, v241\n"
    "v_mfma_f32_16x16x32_bf16 v[212:215], v[188:191], v[12:15], 0\n"
    "ds_read_b128 v[12:15], v2 offset:6144\n"
    "v_mfma_f32_16x16x32_bf16 v[216:219], v[184:187], v[16:19], 0\n"
    "v_rcp_f32 v242, v242\n"
    "v_mfma_f32_16x16x32_bf16 v[220:223], v[188:191], v[16:19], 0\n"
    "ds_read_b128 v[16:19], v3 offset:4096\n"
    "v_rcp_f32 v243, v243\n"
    "s_waitcnt lgkmcnt(3)\n"
    "v_mfma_f32_16x16x32_bf16 v[208:211], v[40:43], v[20:23], v[208:211]\n"
    "v_mfma_f32_16x16x32_bf16 v[212:215], v[44:47], v[20:23], v[212:215]\n"
    "ds_read_b128 v[20:23], v3 offset:6144\n"
    "v_rcp_f32 v248, v248\n"
    "v_mfma_f32_16x16x32_bf16 v[216:219], v[40:43], v[24:27], v[216:219]\n"
    "v_rcp_f32 v249, v249\n"
    "v_mfma_f32_16x16x32_bf16 v[220:223], v[44:47], v[24:27], v[220:223]\n"
    "ds_read_b128 v[24:27], v2 offset:8192\n"
    "s_waitcnt lgkmcnt(3)\n"
    "v_mfma_f32_16x16x32_bf16 v[208:211], v[48:51], v[28:31], v[208:211]\n"
    "v_rcp_f32 v250, v250\n"
    "v_mfma_f32_16x16x32_bf16 v[212:215], v[52:55], v[28:31], v[212:215]\n"
    "ds_read_b128 v[28:31], v2 offset:10240\n"
    "v_mfma_f32_16x16x32_bf16 v[216:219], v[48:51], v[12:15], v[216:219]\n"
    "v_rcp_f32 v251, v251\n"
    "v_mul_f32 v240, v240, v244\n"
    "v_mfma_f32_16x16x32_bf16 v[220:223], v[52:55], v[12:15], v[220:223]\n"
    "ds_read_b128 v[12:15], v3 offset:8192\n"
    "s_waitcnt lgkmcnt(3)\n"
    "v_mfma_f32_16x16x32_bf16 v[208:211], v[56:59], v[16:19], v[208:211]\n"
    "v_mul_f32 v241, v241, v245\n"
    "v_mul_f32 v242, v242, v246\n"
    "v_mfma_f32_16x16x32_bf16 v[212:215], v[60:63], v[16:19], v[212:215]\n"
    "ds_read_b128 v[16:19], v3 offset:10240\n"
    "v_mul_f32 v243, v243, v247\n"
    "v_mfma_f32_16x16x32_bf16 v[216:219], v[56:59], v[20:23], v[216:219]\n"
    "v_mul_f32 v248, v248, v252\n"
    "v_mul_f32 v249, v249, v253\n"
    "v_mfma_f32_16x16x32_bf16 v[220:223], v[60:63], v[20:23], v[220:223]\n"
    "ds_read_b128 v[20:23], v2 offset:12288\n"
    "v_mul_f32 v250, v250, v254\n"
    "s_waitcnt lgkmcnt(3)\n"
    "v_mfma_f32_16x16x32_bf16 v[208:211], v[64:67], v[24:27], v[208:211]\n"
    "v_mul_f32 v251, v251, v255\n"
    "v_mfma_f32_16x16x32_bf16 v[212:215], v[68:71], v[24:27], v[212:215]\n"
    "ds_read_b128 v[24:27], v2 offset:14336\n"
    "v_cvt_pk_bf16_f32 v244, v240, v241\n"
    "v_mfma_f32_16x16x32_bf16 v[216:219], v[64:67], v[28:31], v[216:219]\n"
    "v_cvt_pk_bf16_f32 v245, v242, v243\n"
    "v_cvt_pk_bf16_f32 v246, v248, v249\n"
    "v_mfma_f32_16x16x32_bf16 v[220:223], v[68:71], v[28:31], v[220:223]\n"
    "ds_read_b128 v[28:31], v3 offset:12288\n"
    "v_cvt_pk_bf16_f32 v247, v250, v251\n"
    "s_waitcnt lgkmcnt(3)\n"
    "v_mfma_f32_16x16x32_bf16 v[208:211], v[72:75], v[12:15], v[208:211]\n"
    "s_nop 1\n"
    "v_permlane16_swap_b32 v244, v246\n"
    "v_mfma_f32_16x16x32_bf16 v[212:215], v[76:79], v[12:15], v[212:215]\n"
    "ds_read_b128 v[12:15], v3 offset:14336\n"
    "v_mfma_f32_16x16x32_bf16 v[216:219], v[72:75], v[16:19], v[216:219]\n"
    "v_permlane16_swap_b32 v245, v247\n"
    "buffer_store_dwordx4 v[244:247], v0, s[28:31], s91 offen sc1\n"
    "v_mfma_f32_16x16x32_bf16 v[220:223], v[76:79], v[16:19], v[220:223]\n"
    "ds_read_b128 v[16:19], v2 offset:16384\n"
    "s_add_u32 s91, s90, 393216\n"
    "s_waitcnt lgkmcnt(3)\n"
    "v_mfma_f32_16x16x32_bf16 v[208:211], v[80:83], v[20:23], v[208:211]\n"
    "v_mul_f32 v192, s52, v192\n"
    "v_mul_f32 v193, s52, v193\n"
    "v_mfma_f32_16x16x32_bf16 v[212:215], v[84:87], v[20:23], v[212:215]\n"
    "ds_read_b128 v[20:23], v2 offset:18432\n"
    "v_mfma_f32_16x16x32_bf16 v[216:219], v[80:83], v[24:27], v[216:219]\n"
    "v_mul_f32 v194, s52, v194\n"
    "v_mul_f32 v195, s52, v195\n"
    "v_mfma_f32_16x16x32_bf16 v[220:223], v[84:87], v[24:27], v[220:223]\n"
    "ds_read_b128 v[24:27], v3 offset:16384\n"
    "v_mul_f32 v200, s52, v200\n"
    "s_waitcnt lgkmcnt(3)\n"
    "v_mfma_f32_16x16x32_bf16 v[208:211], v[88:91], v[28:31], v[208:211]\n"
    "v_mul_f32 v201, s52, v201\n"
    "v_mul_f32 v202, s52, v202\n"
    "v_mfma_f32_16x16x32_bf16 v[212:215], v[92:95], v[28:31], v[212:215]\n"
    "ds_read_b128 v[28:31], v3 offset:18432\n"
    "v_mul_f32 v203, s52, v203\n"
    "v_mfma_f32_16x16x32_bf16 v[216:219], v[88:91], v[12:15], v[216:219]\n"
    "v_exp_f32 v192, v192\n"
    "v_mfma_f32_16x16x32_bf16 v[220:223], v[92:95], v[12:15], v[220:223]\n"
    "ds_read_b128 v[12:15], v2 offset:20480\n"
    "s_waitcnt lgkmcnt(3)\n"
    "v_mfma_f32_16x16x32_bf16 v[208:211], v[96:99], v[16:19], v[208:211]\n"
    "v_exp_f32 v193, v193\n"
    "v_mfma_f32_16x16x32_bf16 v[212:215], v[100:103], v[16:19], v[212:215]\n"
    "ds_read_b128 v[16:19], v2 offset:22528\n"
    "v_mfma_f32_16x16x32_bf16 v[216:219], v[96:99], v[20:23], v[216:219]\n"
    "v_exp_f32 v194, v194\n"
    "v_mfma_f32_16x16x32_bf16 v[220:223], v[100:103], v[20:23], v[220:223]\n"
    "ds_read_b128 v[20:23], v3 offset:20480\n"
    "v_exp_f32 v195, v195\n"
    "s_waitcnt lgkmcnt(3)\n"
    "v_mfma_f32_16x16x32_bf16 v[208:211], v[104:107], v[24:27], v[208:211]\n"
    "v_exp_f32 v200, v200\n"
    "v_mfma_f32_16x16x32_bf16 v[212:215], v[108:111], v[24:27], v[212:215]\n"
    "ds_read_b128 v[24:27], v3 offset:22528\n"
    "v_mfma_f32_16x16x32_bf16 v[216:219], v[104:107], v[28:31], v[216:219]\n"
    "v_exp_f32 v201, v201\n"
    "v_mfma_f32_16x16x32_bf16 v[220:223], v[108:111], v[28:31], v[220:223]\n"
    "ds_read_b128 v[28:31], v2 offset:24576\n"
    "s_waitcnt lgkmcnt(3)\n"
    "v_mfma_f32_16x16x32_bf16 v[208:211], v[112:115], v[12:15], v[208:211]\n"
    "v_exp_f32 v202, v202\n"
    "v_mfma_f32_16x16x32_bf16 v[212:215], v[116:119], v[12:15], v[212:215]\n"
    "ds_read_b128 v[12:15], v2 offset:26624\n"
    "v_mfma_f32_16x16x32_bf16 v[216:219], v[112:115], v[16:19], v[216:219]\n"
    "v_exp_f32 v203, v203\n"
    "v_add_f32 v192, 1.0, v192\n"
    "v_mfma_f32_16x16x32_bf16 v[220:223], v[116:119], v[16:19], v[220:223]\n"
    "ds_read_b128 v[16:19], v3 offset:24576\n"
    "v_add_f32 v193, 1.0, v193\n"
    "s_waitcnt lgkmcnt(3)\n"
    "v_mfma_f32_16x16x32_bf16 v[208:211], v[120:123], v[20:23], v[208:211]\n"
    "v_add_f32 v194, 1.0, v194\n"
    "v_mfma_f32_16x16x32_bf16 v[212:215], v[124:127], v[20:23], v[212:215]\n"
    "ds_read_b128 v[20:23], v3 offset:26624\n"
    "v_add_f32 v195, 1.0, v195\n"
    "v_mfma_f32_16x16x32_bf16 v[216:219], v[120:123], v[24:27], v[216:219]\n"
    "v_add_f32 v200, 1.0, v200\n"
    "v_add_f32 v201, 1.0, v201\n"
    "v_mfma_f32_16x16x32_bf16 v[220:223], v[124:127], v[24:27], v[220:223]\n"
    "ds_read_b128 v[24:27], v2 offset:28672\n"
    "v_add_f32 v202, 1.0, v202\n"
    "s_waitcnt lgkmcnt(3)\n"
    "v_mfma_f32_16x16x32_bf16 v[208:211], v[128:131], v[28:31], v[208:211]\n"
    "v_add_f32 v203, 1.0, v203\n"
    "v_mfma_f32_16x16x32_bf16 v[212:215], v[132:135], v[28:31], v[212:215]\n"
    "ds_read_b128 v[28:31], v2 offset:30720\n"
    "v_rcp_f32 v192, v192\n"
    "v_mfma_f32_16x16x32_bf16 v[216:219], v[128:131], v[12:15], v[216:219]\n"
    "v_rcp_f32 v193, v193\n"
    "v_mfma_f32_16x16x32_bf16 v[220:223], v[132:135], v[12:15], v[220:223]\n"
    "ds_read_b128 v[12:15], v3 offset:28672\n"
    "s_waitcnt lgkmcnt(3)\n"
    "v_mfma_f32_16x16x32_bf16 v[208:211], v[136:139], v[16:19], v[208:211]\n"
    "v_rcp_f32 v194, v194\n"
    "v_mfma_f32_16x16x32_bf16 v[212:215], v[140:143], v[16:19], v[212:215]\n"
    "ds_read_b128 v[16:19], v3 offset:30720\n"
    "v_mfma_f32_16x16x32_bf16 v[216:219], v[136:139], v[20:23], v[216:219]\n"
    "v_rcp_f32 v195, v195\n"
    "v_mfma_f32_16x16x32_bf16 v[220:223], v[140:143], v[20:23], v[220:223]\n"
    "ds_read_b128 v[20:23], v2 offset:32768\n"
    "s_waitcnt lgkmcnt(3)\n"
    "v_mfma_f32_16x16x32_bf16 v[208:211], v[144:147], v[24:27], v[208:211]\n"
    "v_rcp_f32 v200, v200\n"
    "v_mfma_f32_16x16x32_bf16 v[212:215], v[148:151], v[24:27], v[212:215]\n"
    "ds_read_b128 v[24:27], v2 offset:34816\n"
    "v_rcp_f32 v201, v201\n"
    "v_mfma_f32_16x16x32_bf16 v[216:219], v[144:147], v[28:31], v[216:219]\n"
    "v_rcp_f32 v202, v202\n"
    "v_mfma_f32_16x16x32_bf16 v[220:223], v[148:151], v[28:31], v[220:223]\n"
    "ds_read_b128 v[28:31], v3 offset:32768\n"
    "s_waitcnt lgkmcnt(3)\n"
    "v_mfma_f32_16x16x32_bf16 v[208:211], v[152:155], v[12:15], v[208:211]\n"
    "v_rcp_f32 v203, v203\n"
    "v_mfma_f32_16x16x32_bf16 v[212:215], v[156:159], v[12:15], v[212:215]\n"
    "ds_read_b128 v[12:15], v3 offset:34816\n"
    "v_mul_f32 v192, v192, v196\n"
    "v_mfma_f32_16x16x32_bf16 v[216:219], v[152:155], v[16:19], v[216:219]\n"
    "v_mul_f32 v193, v193, v197\n"
    "v_mfma_f32_16x16x32_bf16 v[220:223], v[156:159], v[16:19], v[220:223]\n"
    "ds_read_b128 v[16:19], v2 offset:36864\n"
    "v_mul_f32 v194, v194, v198\n"
    "s_waitcnt lgkmcnt(3)\n"
    "v_mfma_f32_16x16x32_bf16 v[208:211], v[160:163], v[20:23], v[208:211]\n"
    "v_mul_f32 v195, v195, v199\n"
    "v_mul_f32 v200, v200, v204\n"
    "v_mfma_f32_16x16x32_bf16 v[212:215], v[164:167], v[20:23], v[212:215]\n"
    "ds_read_b128 v[20:23], v2 offset:38912\n"
    "v_mul_f32 v201, v201, v205\n"
    "v_mfma_f32_16x16x32_bf16 v[216:219], v[160:163], v[24:27], v[216:219]\n"
    "v_mul_f32 v202, v202, v206\n"
    "v_mul_f32 v203, v203, v207\n"
    "v_mfma_f32_16x16x32_bf16 v[220:223], v[164:167], v[24:27], v[220:223]\n"
    "ds_read_b128 v[24:27], v3 offset:36864\n"
    "s_waitcnt lgkmcnt(3)\n"
    "v_mfma_f32_16x16x32_bf16 v[208:211], v[168:171], v[28:31], v[208:211]\n"
    "v_cvt_pk_bf16_f32 v196, v192, v193\n"
    "v_cvt_pk_bf16_f32 v197, v194, v195\n"
    "v_mfma_f32_16x16x32_bf16 v[212:215], v[172:175], v[28:31], v[212:215]\n"
    "ds_read_b128 v[28:31], v3 offset:38912\n"
    "v_cvt_pk_bf16_f32 v198, v200, v201\n"
    "v_mfma_f32_16x16x32_bf16 v[216:219], v[168:171], v[12:15], v[216:219]\n"
    "v_cvt_pk_bf16_f32 v199, v202, v203\n"
    "s_nop 1\n"
    "v_mfma_f32_16x16x32_bf16 v[220:223], v[172:175], v[12:15], v[220:223]\n"
    "s_waitcnt lgkmcnt(2)\n"
    "v_mfma_f32_16x16x32_bf16 v[208:211], v[176:179], v[16:19], v[208:211]\n"
    "v_permlane16_swap_b32 v196, v198\n"
    "v_permlane16_swap_b32 v197, v199\n"
    "v_mfma_f32_16x16x32_bf16 v[212:215], v[180:183], v[16:19], v[212:215]\n"
    "buffer_store_dwordx4 v[196:199], v0, s[28:31], s91 offen sc1\n"
    "v_mfma_f32_16x16x32_bf16 v[216:219], v[176:179], v[20:23], v[216:219]\n"
    "v_mfma_f32_16x16x32_bf16 v[220:223], v[180:183], v[20:23], v[220:223]\n"
    "s_waitcnt lgkmcnt(0)\n"
    "v_mfma_f32_16x16x32_bf16 v[208:211], v[32:35], v[24:27], v[208:211]\n"
    "v_mfma_f32_16x16x32_bf16 v[212:215], v[36:39], v[24:27], v[212:215]\n"
    "v_mfma_f32_16x16x32_bf16 v[216:219], v[32:35], v[28:31], v[216:219]\n"
    "v_mfma_f32_16x16x32_bf16 v[220:223], v[36:39], v[28:31], v[220:223]\n"
    "s_lshl_b32 s48, s59, 12\n"
    "v_add_u32 v240, s48, v7\n"
    "s_lshl_b32 s48, s59, 11\n"
    "s_sub_u32 s48, 1024, s48\n"
    "v_add_u32 v241, s48, v240\n"
    "v_add_u32 v242, s48, v241\n"
    "v_add_u32 v243, s48, v242\n"
    "v_add_u32 v244, s48, v243\n"
    "s_add_u32 s91, s90, 491520\n"
    "v_pk_mul_f32 v[208:209], v[208:209], s[52:53]\n"
    "v_pk_mul_f32 v[210:211], v[210:211], s[52:53]\n"
    "v_pk_mul_f32 v[216:217], v[216:217], s[52:53]\n"
    "v_pk_mul_f32 v[218:219], v[218:219], s[52:53]\n"
    "v_exp_f32 v208, v208\n"
    "v_exp_f32 v209, v209\n"
    "v_exp_f32 v210, v210\n"
    "v_exp_f32 v211, v211\n"
    "v_exp_f32 v216, v216\n"
    "v_exp_f32 v217, v217\n"
    "v_exp_f32 v218, v218\n"
    "v_exp_f32 v219, v219\n"
    "v_pk_add_f32 v[208:209], v[208:209], 1.0 op_sel_hi:[1,0]\n"
    "v_pk_add_f32 v[210:211], v[210:211], 1.0 op_sel_hi:[1,0]\n"
    "v_pk_add_f32 v[216:217], v[216:217], 1.0 op_sel_hi:[1,0]\n"
    "v_pk_add_f32 v[218:219], v[218:219], 1.0 op_sel_hi:[1,0]\n"
    "v_rcp_f32 v208, v208\n"
    "v_rcp_f32 v209, v209\n"
    "v_rcp_f32 v210, v210\n"
    "v_rcp_f32 v211, v211\n"
    "v_rcp_f32 v216, v216\n"
    "v_rcp_f32 v217, v217\n"
    "v_rcp_f32 v218, v218\n"
    "v_rcp_f32 v219, v219\n"
    "v_pk_mul_f32 v[208:209], v[208:209], v[212:213]\n"
    "v_pk_mul_f32 v[210:211], v[210:211], v[214:215]\n"
    "v_pk_mul_f32 v[216:217], v[216:217], v[220:221]\n"
    "v_pk_mul_f32 v[218:219], v[218:219], v[222:223]\n"
    "v_cvt_pk_bf16_f32 v212, v208, v209\n"
    "v_cvt_pk_bf16_f32 v213, v210, v211\n"
    "v_cvt_pk_bf16_f32 v214, v216, v217\n"
    "v_cvt_pk_bf16_f32 v215, v218, v219\n"
    "s_nop 1\n"
    "v_permlane16_swap_b32 v212, v214\n"
    "v_permlane16_swap_b32 v213, v215\n"
    "buffer_store_dwordx4 v[212:215], v0, s[28:31], s91 offen sc1\n"
    "s_add_u32 s58, s58, 0\n"
    "buffer_load_dwordx4 v[16:19], v240, s[28:31], s58 offen sc1\n"
    "buffer_load_dwordx4 v[20:23], v241, s[28:31], s58 offen sc1\n"
    "buffer_load_dwordx4 v[24:27], v242, s[28:31], s58 offen sc1\n"
    "buffer_load_dwordx4 v[28:31], v243, s[28:31], s58 offen sc1\n"
    "buffer_load_dwordx4 v[32:35], v244, s[28:31], s58 offen sc1\n"
    "s_add_u32 s58, s58, 5120\n"
    "v_add_u32 v224, s82, v7\n"
    "v_add_u32 v225, 122880, v224\n"
    "v_add_u32 v226, s83, v7\n"
    "v_add_u32 v227, 122880, v226\n"
    "v_add_u32 v228, s84, v7\n"
    "v_add_u32 v229, s85, v7\n"
    "v_add_u32 v233, s86, v7\n"
    "v_and_b32 v9, 15, v1\n"
    "v_mul_u32_u24 v9, 1280, v9\n"
    "v_and_b32 v10, 48, v1\n"
    "v_add_u32 v231, v9, v10\n"
    "v_lshrrev_b32 v10, 1, v10\n"
    "v_add_u32 v232, v9, v10\n"
    "v_add_u32 v8, 4096, v7\n"
    "s_mul_i32 s48, s3, 0\n"
    "s_add_u32 s48, s48, 40960\n"
    "v_add_u32 v9, s48, v7\n"
    "s_mov_b64 vcc, -1\n"
    "s_waitcnt vmcnt(5)\n"
    "s_barrier\n"
    "buffer_load_dwordx4 v[36:39], v240, s[28:31], s58 offen sc1\n"
    "buffer_load_dwordx4 v[40:43], v241, s[28:31], s58 offen sc1\n"
    "buffer_load_dwordx4 v[44:47], v242, s[28:31], s58 offen sc1\n"
    "buffer_load_dwordx4 v[48:51], v243, s[28:31], s58 offen sc1\n"
    "buffer_load_dwordx4 v[52:55], v244, s[28:31], s58 offen sc1\n"
    "s_add_u32 s58, s58, 5120\n"
    "buffer_load_dwordx4 v[56:59], v240, s[28:31], s58 offen sc1\n"
    "buffer_load_dwordx4 v[60:63], v241, s[28:31], s58 offen sc1\n"
    "buffer_load_dwordx4 v[64:67], v242, s[28:31], s58 offen sc1\n"
    "buffer_load_dwordx4 v[68:71], v243, s[28:31], s58 offen sc1\n"
    "buffer_load_dwordx4 v[72:75], v244, s[28:31], s58 offen sc1\n"
    "s_add_u32 s58, s58, 5120\n"
    "buffer_load_dwordx4 v[76:79], v240, s[28:31], s58 offen sc1\n"
    "buffer_load_dwordx4 v[80:83], v241, s[28:31], s58 offen sc1\n"
    "buffer_load_dwordx4 v[84:87], v242, s[28:31], s58 offen sc1\n"
    "buffer_load_dwordx4 v[88:91], v243, s[28:31], s58 offen sc1\n"
    "buffer_load_dwordx4 v[92:95], v244, s[28:31], s58 offen sc1\n"
    "s_add_u32 s58, s58, 5120\n"
    "buffer_load_dwordx4 v[96:99], v240, s[28:31], s58 offen sc1\n"
    "buffer_load_dwordx4 v[100:103], v241, s[28:31], s58 offen sc1\n"
    "buffer_load_dwordx4 v[104:107], v242, s[28:31], s58 offen sc1\n"
    "buffer_load_dwordx4 v[108:111], v243, s[28:31], s58 offen sc1\n"
    "buffer_load_dwordx4 v[112:115], v244, s[28:31], s58 offen sc1\n"
    "s_add_u32 s58, s58, 5120\n"
    "buffer_load_dwordx4 v[116:119], v240, s[28:31], s58 offen sc1\n"
    "buffer_load_dwordx4 v[120:123], v241, s[28:31], s58 offen sc1\n"
    "buffer_load_dwordx4 v[124:127], v242, s[28:31], s58 offen sc1\n"
    "buffer_load_dwordx4 v[128:131], v243, s[28:31], s58 offen sc1\n"
    "buffer_load_dwordx4 v[132:135], v244, s[28:31], s58 offen sc1\n"
    "s_branch HOK_%=\n"
    "HOOK1_%=:\n"
    "s_waitcnt vmcnt(0)\n"
    "ds_read_b64_tr_b16 v[164:165], v161 offset:0\n"
    "ds_read_b64_tr_b16 v[166:167], v161 offset:640\n"
    "ds_read_b64_tr_b16 v[168:169], v161 offset:8\n"
    "ds_read_b64_tr_b16 v[170:171], v161 offset:648\n"
    "ds_read_b64_tr_b16 v[172:173], v162 offset:64\n"
    "ds_read_b64_tr_b16 v[174:175], v162 offset:704\n"
    "ds_read_b64_tr_b16 v[176:177], v161 offset:104\n"
    "ds_read_b64_tr_b16 v[178:179], v161 offset:744\n"
    "ds_read_b64_tr_b16 v[180:181], v161 offset:96\n"
    "ds_read_b64_tr_b16 v[182:183], v161 offset:736\n"
    "s_waitcnt lgkmcnt(0)\n"
    "buffer_store_dwordx4 v[164:167], v7, s[28:31], s40 offen offset:0 sc1\n"
    "buffer_store_dwordx4 v[168:171], v7, s[28:31], s40 offen offset:1024 sc1\n"
    "buffer_store_dwordx4 v[172:175], v7, s[28:31], s40 offen offset:2048 sc1\n"
    "buffer_store_dwordx4 v[176:179], v7, s[28:31], s40 offen offset:3072 sc1\n"
    "buffer_store_dwordx4 v[180:183], v156, s[28:31], s40 offen sc1\n"
    "s_add_u32 s32, s32, 1\n"
    "s_addc_u32 s33, s33, 0\n"
    "s_branch HBACK1_%=\n"
    "HOOK2_%=:\n"
    "s_waitcnt vmcnt(0)\n"
    "v_mov_b32 v10, s32\n"
    "v_mov_b32 v11, s33\n"
    "v_mov_b32 v9, 0\n"
    "s_mov_b64 exec, 1\n"
    "buffer_store_dwordx2 v[10:11], v9, s[28:31], s62 offen sc1\n"
    "s_mov_b64 exec, -1\n"
    "s_branch HBACK2_%=\n"
    ".p2align 12\n"
    "PAD3_%=:\n"
    "s_cbranch_execz PADRET_%=\n"
    "HOK_%=:\n"
    "s_barrier\n"
    "buffer_load_dwordx4 v[136:139], v7, s[28:31], s54 offen offset:0 sc1\n"
    "buffer_load_dwordx4 v[140:143], v7, s[28:31], s54 offen offset:1024 sc1\n"
    "buffer_load_dwordx4 v[144:147], v7, s[28:31], s54 offen offset:2048 sc1\n"
    "buffer_load_dwordx4 v[148:151], v7, s[28:31], s54 offen offset:3072 sc1\n"
    "buffer_load_dwordx4 v[152:155], v8, s[28:31], s54 offen offset:0 sc1\n"
    "s_waitcnt vmcnt(4)\n"
    "v_mfma_f32_16x16x32_bf16 v[184:187], v[16:19], v[136:139], 0\n"
    "s_setprio 1\n"
    "v_mfma_f32_16x16x32_bf16 v[188:191], v[20:23], v[136:139], 0\n"
    "v_mfma_f32_16x16x32_bf16 v[192:195], v[24:27], v[136:139], 0\n"
    "v_mfma_f32_16x16x32_bf16 v[196:199], v[28:31], v[136:139], 0\n"
    "v_mfma_f32_16x16x32_bf16 v[200:203], v[32:35], v[136:139], 0\n"
    "s_setprio 0\n"
    "buffer_load_dwordx4 v[156:159], v8, s[28:31], s54 offen offset:1024 sc1\n"
    "s_waitcnt vmcnt(4)\n"
    "v_mfma_f32_16x16x32_bf16 v[184:187], v[36:39], v[140:143], v[184:187]\n"
    "s_setprio 1\n"
    "v_mfma_f32_16x16x32_bf16 v[188:191], v[40:43], v[140:143], v[188:191]\n"
    "v_mfma_f32_16x16x32_bf16 v[192:195], v[44:47], v[140:143], v[192:195]\n"
    "v_mfma_f32_16x16x32_bf16 v[196:199], v[48:51], v[140:143], v[196:199]\n"
    "v_mfma_f32_16x16x32_bf16 v[200:203], v[52:55], v[140:143], v[200:203]\n"
    "s_setprio 0\n"
    "buffer_load_dwordx4 v[160:163], v7, s[28:31], s55 offen offset:0 sc1\n"
    "s_waitcnt vmcnt(4)\n"
    "v_mfma_f32_16x16x32_bf16 v[184:187], v[56:59], v[144:147], v[184:187]\n"
    "s_setprio 1\n"
    "v_mfma_f32_16x16x32_bf16 v[188:191], v[60:63], v[144:147], v[188:191]\n"
    "v_mfma_f32_16x16x32_bf16 v[192:195], v[64:67], v[144:147], v[192:195]\n"
    "v_mfma_f32_16x16x32_bf16 v[196:199], v[68:71], v[144:147], v[196:199]\n"
    "v_mfma_f32_16x16x32_bf16 v[200:203], v[72:75], v[144:147], v[200:203]\n"
    "s_setprio 0\n"
    "buffer_load_dwordx4 v[164:167], v7, s[28:31], s55 offen offset:1024 sc1\n"
    "s_waitcnt vmcnt(4)\n"
    "v_mfma_f32_16x16x32_bf16 v[184:187], v[76:79], v[148:151], v[184:187]\n"
    "s_setprio 1\n"
    "v_mfma_f32_16x16x32_bf16 v[188:191], v[80:83], v[148:151], v[188:191]\n"
    "v_mfma_f32_16x16x32_bf16 v[192:195], v[84:87], v[148:151], v[192:195]\n"
    "v_mfma_f32_16x16x32_bf16 v[196:199], v[88:91], v[148:151], v[196:199]\n"
    "v_mfma_f32_16x16x32_bf16 v[200:203], v[92:95], v[148:151], v[200:203]\n"
    "s_setprio 0\n"
    "buffer_load_dwordx4 v[168:171], v7, s[28:31], s55 offen offset:2048 sc1\n"
    "s_waitcnt vmcnt(4)\n"
    "v_mfma_f32_16x16x32_bf16 v[184:187], v[96:99], v[152:155], v[184:187]\n"
    "s_setprio 1\n"
    "v_mfma_f32_16x16x32_bf16 v[188:191], v[100:103], v[152:155], v[188:191]\n"
    "v_mfma_f32_16x16x32_bf16 v[192:195], v[104:107], v[152:155], v[192:195]\n"
    "v_mfma_f32_16x16x32_bf16 v[196:199], v[108:111], v[152:155], v[196:199]\n"
    "v_mfma_f32_16x16x32_bf16 v[200:203], v[112:115], v[152:155], v[200:203]\n"
    "s_setprio 0\n"
    "buffer_load_dwordx4 v[172:175], v7, s[28:31], s55 offen offset:3072 sc1\n"
    "s_waitcnt vmcnt(4)\n"
    "v_mfma_f32_16x16x32_bf16 v[184:187], v[116:119], v[156:159], v[184:187]\n"
    "s_setprio 1\n"
    "v_mfma_f32_16x16x32_bf16 v[188:191], v[120:123], v[156:159], v[188:191]\n"
    "v_mfma_f32_16x16x32_bf16 v[192:195], v[124:127], v[156:159], v[192:195]\n"
    "v_mfma_f32_16x16x32_bf16 v[196:199], v[128:131], v[156:159], v[196:199]\n"
    "v_mfma_f32_16x16x32_bf16 v[200:203], v[132:135], v[156:159], v[200:203]\n"
    "s_setprio 0\n"
    "buffer_load_dwordx4 v[176:179], v8, s[28:31], s55 offen offset:0 sc1\n"
    "s_waitcnt vmcnt(4)\n"
    "v_mfma_f32_16x16x32_bf16 v[204:207], v[16:19], v[160:163], 0\n"
    "s_setprio 1\n"
    "v_mfma_f32_16x16x32_bf16 v[208:211], v[20:23], v[160:163], 0\n"
    "v_mfma_f32_16x16x32_bf16 v[212:215], v[24:27], v[160:163], 0\n"
    "v_mfma_f32_16x16x32_bf16 v[216:219], v[28:31], v[160:163], 0\n"
    "ds_write_b128 v225, v[184:187] offset:0\n"
    "v_mfma_f32_16x16x32_bf16 v[220:223], v[32:35], v[160:163], 0\n"
    "s_setprio 0\n"
    "buffer_load_dwordx4 v[180:183], v8, s[28:31], s55 offen offset:1024 sc1\n"
    "s_waitcnt vmcnt(4)\n"
    "v_mfma_f32_16x16x32_bf16 v[204:207], v[36:39], v[164:167], v[204:207]\n"
    "s_setprio 1\n"
    "v_mfma_f32_16x16x32_bf16 v[208:211], v[40:43], v[164:167], v[208:211]\n"
    "ds_write_b128 v225, v[188:191] offset:1024\n"
    "v_mfma_f32_16x16x32_bf16 v[212:215], v[44:47], v[164:167], v[212:215]\n"
    "v_mfma_f32_16x16x32_bf16 v[216:219], v[48:51], v[164:167], v[216:219]\n"
    "v_mfma_f32_16x16x32_bf16 v[220:223], v[52:55], v[164:167], v[220:223]\n"
    "s_setprio 0\n"
    "ds_write_b128 v225, v[192:195] offset:2048\n"
    "buffer_load_dwordx4 v[136:139], v7, s[28:31], s56 offen offset:0 sc1\n"
    "s_waitcnt vmcnt(4)\n"
    "v_mfma_f32_16x16x32_bf16 v[204:207], v[56:59], v[168:171], v[204:207]\n"
    "s_setprio 1\n"
    "v_mfma_f32_16x16x32_bf16 v[208:211], v[60:63], v[168:171], v[208:211]\n"
    "v_mfma_f32_16x16x32_bf16 v[212:215], v[64:67], v[168:171], v[212:215]\n"
    "ds_write_b128 v225, v[196:199] offset:3072\n"
    "v_mfma_f32_16x16x32_bf16 v[216:219], v[68:71], v[168:171], v[216:219]\n"
    "v_mfma_f32_16x16x32_bf16 v[220:223], v[72:75], v[168:171], v[220:223]\n"
    "s_setprio 0\n"
    "buffer_load_dwordx4 v[140:143], v7, s[28:31], s56 offen offset:1024 sc1\n"
    "s_waitcnt vmcnt(4)\n"
    "v_mfma_f32_16x16x32_bf16 v[204:207], v[76:79], v[172:175], v[204:207]\n"
    "s_setprio 1\n"
    "ds_write_b128 v225, v[200:203] offset:4096\n"
    "v_mfma_f32_16x16x32_bf16 v[208:211], v[80:83], v[172:175], v[208:211]\n"
    "v_mfma_f32_16x16x32_bf16 v[212:215], v[84:87], v[172:175], v[212:215]\n"
    "v_mfma_f32_16x16x32_bf16 v[216:219], v[88:91], v[172:175], v[216:219]\n"
    "v_mfma_f32_16x16x32_bf16 v[220:223], v[92:95], v[172:175], v[220:223]\n"
    "s_setprio 0\n"
    "buffer_load_dwordx4 v[144:147], v7, s[28:31], s56 offen offset:2048 sc1\n"
    "s_waitcnt lgkmcnt(0)\n"
    "s_barrier\n"
    "s_waitcnt vmcnt(4)\n"
    "v_mfma_f32_16x16x32_bf16 v[204:207], v[96:99], v[176:179], v[204:207]\n"
    "s_setprio 1\n"
    "ds_read_b128 v[184:187], v227 offset:0\n"
    "v_mfma_f32_16x16x32_bf16 v[208:211], v[100:103], v[176:179], v[208:211]\n"
    "ds_read_b128 v[188:191], v227 offset:1024\n"
    "v_mfma_f32_16x16x32_bf16 v[212:215], v[104:107], v[176:179], v[212:215]\n"
    "ds_read_b128 v[192:195], v227 offset:2048\n"
    "v_mfma_f32_16x16x32_bf16 v[216:219], v[108:111], v[176:179], v[216:219]\n"
    "ds_read_b128 v[196:199], v227 offset:3072\n"
    "v_mfma_f32_16x16x32_bf16 v[220:223], v[112:115], v[176:179], v[220:223]\n"
    "s_setprio 0\n"
    "ds_read_b128 v[200:203], v227 offset:4096\n"
    "buffer_load_dwordx4 v[148:151], v7, s[28:31], s56 offen offset:3072 sc1\n"
    "s_waitcnt vmcnt(4)\n"
    "v_mfma_f32_16x16x32_bf16 v[204:207], v[116:119], v[180:183], v[204:207]\n"
    "s_setprio 1\n"
    "v_mfma_f32_16x16x32_bf16 v[208:211], v[120:123], v[180:183], v[208:211]\n"
    "v_mfma_f32_16x16x32_bf16 v[212:215], v[124:127], v[180:183], v[212:215]\n"
    "v_mfma_f32_16x16x32_bf16 v[216:219], v[128:131], v[180:183], v[216:219]\n"
    "v_mfma_f32_16x16x32_bf16 v[220:223], v[132:135], v[180:183], v[220:223]\n"
    "s_setprio 0\n"
    "buffer_load_dwordx4 v[152:155], v8, s[28:31], s56 offen offset:0 sc1\n"
    "s_waitcnt vmcnt(4) lgkmcnt(0)\n"
    "v_mfma_f32_16x16x32_bf16 v[184:187], v[16:19], v[136:139], v[184:187]\n"
    "s_setprio 1\n"
    "v_mfma_f32_16x16x32_bf16 v[188:191], v[20:23], v[136:139], v[188:191]\n"
    "v_mfma_f32_16x16x32_bf16 v[192:195], v[24:27], v[136:139], v[192:195]\n"
    "v_mfma_f32_16x16x32_bf16 v[196:199], v[28:31], v[136:139], v[196:199]\n"
    "ds_write_b128 v224, v[204:207] offset:0\n"
    "v_mfma_f32_16x16x32_bf16 v[200:203], v[32:35], v[136:139], v[200:203]\n"
    "s_setprio 0\n"
    "buffer_load_dwordx4 v[156:159], v8, s[28:31], s56 offen offset:1024 sc1\n"
    "s_waitcnt vmcnt(4)\n"
    "v_mfma_f32_16x16x32_bf16 v[184:187], v[36:39], v[140:143], v[184:187]\n"
    "s_setprio 1\n"
    "v_mfma_f32_16x16x32_bf16 v[188:191], v[40:43], v[140:143], v[188:191]\n"
    "ds_write_b128 v224, v[208:211] offset:1024\n"
    "v_mfma_f32_16x16x32_bf16 v[192:195], v[44:47], v[140:143], v[192:195]\n"
    "v_mfma_f32_16x16x32_bf16 v[196:199], v[48:51], v[140:143], v[196:199]\n"
    "v_mfma_f32_16x16x32_bf16 v[200:203], v[52:55], v[140:143], v[200:203]\n"
    "s_setprio 0\n"
    "ds_write_b128 v224, v[212:215] offset:2048\n"
    "buffer_load_dwordx4 v[160:163], v7, s[28:31], s57 offen offset:0 sc1\n"
    "s_waitcnt vmcnt(4)\n"
    "v_mfma_f32_16x16x32_bf16 v[184:187], v[56:59], v[144:147], v[184:187]\n"
    "s_setprio 1\n"
    "v_mfma_f32_16x16x32_bf16 v[188:191], v[60:63], v[144:147], v[188:191]\n"
    "v_mfma_f32_16x16x32_bf16 v[192:195], v[64:67], v[144:147], v[192:195]\n"
    "ds_write_b128 v224, v[216:219] offset:3072\n"
    "v_mfma_f32_16x16x32_bf16 v[196:199], v[68:71], v[144:147], v[196:199]\n"
    "v_mfma_f32_16x16x32_bf16 v[200:203], v[72:75], v[144:147], v[200:203]\n"
    "s_setprio 0\n"
    "buffer_load_dwordx4 v[164:167], v7, s[28:31], s57 offen offset:1024 sc1\n"
    "s_waitcnt vmcnt(4)\n"
    "v_mfma_f32_16x16x32_bf16 v[184:187], v[76:79], v[148:151], v[184:187]\n"
    "s_setprio 1\n"
    "ds_write_b128 v224, v[220:223] offset:4096\n"
    "v_mfma_f32_16x16x32_bf16 v[188:191], v[80:83], v[148:151], v[188:191]\n"
    "v_mfma_f32_16x16x32_bf16 v[192:195], v[84:87], v[148:151], v[192:195]\n"
    "v_mfma_f32_16x16x32_bf16 v[196:199], v[88:91], v[148:151], v[196:199]\n"
    "v_mfma_f32_16x16x32_bf16 v[200:203], v[92:95], v[148:151], v[200:203]\n"
    "s_setprio 0\n"
    "buffer_load_dwordx4 v[168:171], v7, s[28:31], s57 offen offset:2048 sc1\n"
    "s_waitcnt lgkmcnt(0)\n"
    "s_barrier\n"
    "s_waitcnt vmcnt(4)\n"
    "v_mfma_f32_16x16x32_bf16 v[184:187], v[96:99], v[152:155], v[184:187]\n"
    "s_setprio 1\n"
    "ds_read_b128 v[204:207], v226 offset:0\n"
    "v_mfma_f32_16x16x32_bf16 v[188:191], v[100:103], v[152:155], v[188:191]\n"
    "ds_read_b128 v[208:211], v226 offset:1024\n"
    "v_mfma_f32_16x16x32_bf16 v[192:195], v[104:107], v[152:155], v[192:195]\n"
    "ds_read_b128 v[212:215], v226 offset:2048\n"
    "v_mfma_f32_16x16x32_bf16 v[196:199], v[108:111], v[152:155], v[196:199]\n"
    "ds_read_b128 v[216:219], v226 offset:3072\n"
    "v_mfma_f32_16x16x32_bf16 v[200:203], v[112:115], v[152:155], v[200:203]\n"
    "s_setprio 0\n"
    "ds_read_b128 v[220:223], v226 offset:4096\n"
    "buffer_load_dwordx4 v[172:175], v7, s[28:31], s57 offen offset:3072 sc1\n"
    "s_waitcnt vmcnt(4)\n"
    "v_mfma_f32_16x16x32_bf16 v[184:187], v[116:119], v[156:159], v[184:187]\n"
    "s_setprio 1\n"
    "v_mfma_f32_16x16x32_bf16 v[188:191], v[120:123], v[156:159], v[188:191]\n"
    "v_mfma_f32_16x16x32_bf16 v[192:195], v[124:127], v[156:159], v[192:195]\n"
    "v_mfma_f32_16x16x32_bf16 v[196:199], v[128:131], v[156:159], v[196:199]\n"
    "v_mfma_f32_16x16x32_bf16 v[200:203], v[132:135], v[156:159], v[200:203]\n"
    "s_setprio 0\n"
    "buffer_load_dwordx4 v[176:179], v8, s[28:31], s57 offen offset:0 sc1\n"
    "s_waitcnt vmcnt(4) lgkmcnt(0)\n"
    "v_mfma_f32_16x16x32_bf16 v[212:215], v[24:27], v[160:163], v[212:215]\n"
    "v_mfma_f32_16x16x32_bf16 v[216:219], v[28:31], v[160:163], v[216:219]\n"
    "v_mfma_f32_16x16x32_bf16 v[220:223], v[32:35], v[160:163], v[220:223]\n"
    "buffer_load_dwordx4 v[180:183], v8, s[28:31], s57 offen offset:1024 sc1\n"
    "s_waitcnt vmcnt(4)\n"
    "v_mfma_f32_16x16x32_bf16 v[212:215], v[44:47], v[164:167], v[212:215]\n"
    "v_mfma_f32_16x16x32_bf16 v[216:219], v[48:51], v[164:167], v[216:219]\n"
    "ds_write_b128 v224, v[184:187] offset:40960\n"
    "v_mfma_f32_16x16x32_bf16 v[220:223], v[52:55], v[164:167], v[220:223]\n"
    "s_waitcnt vmcnt(3)\n"
    "v_mfma_f32_16x16x32_bf16 v[212:215], v[64:67], v[168:171], v[212:215]\n"
    "v_mfma_f32_16x16x32_bf16 v[216:219], v[68:71], v[168:171], v[216:219]\n"
    "ds_write_b128 v224, v[188:191] offset:41984\n"
    "v_mfma_f32_16x16x32_bf16 v[220:223], v[72:75], v[168:171], v[220:223]\n"
    "s_waitcnt vmcnt(2)\n"
    "v_mfma_f32_16x16x32_bf16 v[212:215], v[84:87], v[172:175], v[212:215]\n"
    "v_mfma_f32_16x16x32_bf16 v[216:219], v[88:91], v[172:175], v[216:219]\n"
    "ds_write_b128 v224, v[192:195] offset:43008\n"
    "v_mfma_f32_16x16x32_bf16 v[220:223], v[92:95], v[172:175], v[220:223]\n"
    "s_waitcnt vmcnt(1)\n"
    "v_mfma_f32_16x16x32_bf16 v[212:215], v[104:107], v[176:179], v[212:215]\n"
    "v_mfma_f32_16x16x32_bf16 v[216:219], v[108:111], v[176:179], v[216:219]\n"
    "ds_write_b128 v224, v[196:199] offset:44032\n"
    "v_mfma_f32_16x16x32_bf16 v[220:223], v[112:115], v[176:179], v[220:223]\n"
    "s_lshl3_add_u32 s51, s2, s3\n"
    "s_cmp_eq_u32 s51, 1920\n"
    "s_waitcnt vmcnt(0)\n"
    "v_mfma_f32_16x16x32_bf16 v[212:215], v[124:127], v[180:183], v[212:215]\n"
    "v_mfma_f32_16x16x32_bf16 v[216:219], v[128:131], v[180:183], v[216:219]\n"
    "ds_write_b128 v224, v[200:203] offset:45056\n"
    "v_mfma_f32_16x16x32_bf16 v[220:223], v[132:135], v[180:183], v[220:223]\n"
    "v_add_u32 v224, 81920, v224\n"
    "s_cbranch_scc1 EPQ_%=\n"
    "EPB_%=:\n"
    "v_mfma_f32_16x16x32_bf16 v[204:207], v[16:19], v[160:163], v[204:207]\n"
    "v_mfma_f32_16x16x32_bf16 v[208:211], v[20:23], v[160:163], v[208:211]\n"
    "s_nop 3\n"
    "ds_write_b128 v224, v[212:215]\n"
    "ds_write_b128 v224, v[216:219] offset:1024\n"
    "v_mfma_f32_16x16x32_bf16 v[204:207], v[36:39], v[164:167], v[204:207]\n"
    "ds_write_b128 v224, v[220:223] offset:2048\n"
    "v_mfma_f32_16x16x32_bf16 v[208:211], v[40:43], v[164:167], v[208:211]\n"
    "v_mfma_f32_16x16x32_bf16 v[204:207], v[56:59], v[168:171], v[204:207]\n"
    "v_mfma_f32_16x16x32_bf16 v[208:211], v[60:63], v[168:171], v[208:211]\n"
    "s_waitcnt lgkmcnt(0)\n"
    "s_barrier\n"
    "ds_read_b128 v[136:139], v228 offset:40960\n"
    "ds_read_b128 v[148:151], v229 offset:45056\n"
    "ds_read_b128 v[184:187], v233 offset:2048\n"
    "v_mfma_f32_16x16x32_bf16 v[204:207], v[76:79], v[172:175], v[204:207]\n"
    "ds_read_b128 v[140:143], v228 offset:41984\n"
    "ds_read_b128 v[152:155], v229 offset:44032\n"
    "ds_read_b128 v[188:191], v233 offset:1024\n"
    "v_mfma_f32_16x16x32_bf16 v[208:211], v[80:83], v[172:175], v[208:211]\n"
    "ds_read_b128 v[144:147], v228 offset:43008\n"
    "ds_read_b128 v[156:159], v229 offset:43008\n"
    "ds_read_b128 v[192:195], v233 offset:0\n"
    "v_mfma_f32_16x16x32_bf16 v[204:207], v[96:99], v[176:179], v[204:207]\n"
    "v_mfma_f32_16x16x32_bf16 v[208:211], v[100:103], v[176:179], v[208:211]\n"
    "v_mfma_f32_16x16x32_bf16 v[204:207], v[116:119], v[180:183], v[204:207]\n"
    "v_mfma_f32_16x16x32_bf16 v[208:211], v[120:123], v[180:183], v[208:211]\n"
    "s_waitcnt lgkmcnt(6)\n"
    "v_pk_add_f32 v[136:137], v[136:137], v[148:149]\n"
    "v_pk_add_f32 v[138:139], v[138:139], v[150:151]\n"
    "v_pk_add_f32 v[136:137], v[136:137], v[184:185]\n"
    "v_pk_add_f32 v[138:139], v[138:139], v[186:187]\n"
    "s_waitcnt lgkmcnt(3)\n"
    "v_pk_add_f32 v[140:141], v[140:141], v[152:153]\n"
    "v_pk_add_f32 v[142:143], v[142:143], v[154:155]\n"
    "v_pk_add_f32 v[140:141], v[140:141], v[188:189]\n"
    "v_pk_add_f32 v[142:143], v[142:143], v[190:191]\n"
    "s_waitcnt lgkmcnt(0)\n"
    "v_pk_add_f32 v[144:145], v[144:145], v[156:157]\n"
    "v_pk_add_f32 v[146:147], v[146:147], v[158:159]\n"
    "v_pk_add_f32 v[144:145], v[144:145], v[192:193]\n"
    "v_pk_add_f32 v[146:147], v[146:147], v[194:195]\n"
    "v_pk_add_f32 v[136:137], v[136:137], v[204:205]\n"
    "v_pk_add_f32 v[138:139], v[138:139], v[206:207]\n"
    "v_pk_add_f32 v[140:141], v[140:141], v[208:209]\n"
    "v_pk_add_f32 v[142:143], v[142:143], v[210:211]\n"
    "v_pk_add_f32 v[144:145], v[144:145], v[212:213]\n"
    "v_pk_add_f32 v[146:147], v[146:147], v[214:215]\n"
    "v_cvt_pk_bf16_f32 v148, v136, v137\n"
    "v_cvt_pk_bf16_f32 v149, v138, v139\n"
    "v_cvt_pk_bf16_f32 v150, v140, v141\n"
    "v_cvt_pk_bf16_f32 v151, v142, v143\n"
    "v_cvt_pk_bf16_f32 v152, v144, v145\n"
    "v_cvt_pk_bf16_f32 v153, v146, v147\n"
    "buffer_store_dwordx4 v[148:151], v231, s[24:27], s87 offen \n"
    "s_cmp_eq_u32 s59, 1\n"
    "s_cbranch_scc1 END_%=\n"
    "buffer_store_dwordx2 v[152:153], v232, s[24:27], s88 offen offset:64 \n"
    "s_cmp_lg_u32 s51, 1920\n"
    "s_cbranch_scc1 END_%=\n"
    "s_waitcnt vmcnt(2)\n"
    "s_mov_b64 vcc, -1\n"
    "v_cmp_eq_u64 s[66:67], s[32:33], v[240:241]\n"
    "s_and_b64 vcc, vcc, s[66:67]\n"
    "v_cmp_eq_u64 s[66:67], s[32:33], v[242:243]\n"
    "s_and_b64 vcc, vcc, s[66:67]\n"
    "v_cmp_eq_u64 s[66:67], s[32:33], v[244:245]\n"
    "s_and_b64 vcc, vcc, s[66:67]\n"
    "v_cmp_eq_u64 s[66:67], s[32:33], v[246:247]\n"
    "s_and_b64 vcc, vcc, s[66:67]\n"
    "v_cmp_eq_u64 s[66:67], s[32:33], v[248:249]\n"
    "s_and_b64 vcc, vcc, s[66:67]\n"
    "s_cmp_eq_u64 vcc, -1\n"
    "s_cbranch_scc1 EPOK_%=\n"
    "EPL_%=:\n"
    "s_mov_b32 s45, 1048576\n"
    "PL7_%=:\n"
    "buffer_load_dwordx2 v[236:237], v230, s[28:31], s49 offen sc1\n"
    "s_sub_u32 s45, s45, 1\n"
    "s_cbranch_scc1 PL7x_%=\n"
    "s_waitcnt vmcnt(0)\n"
    "v_cmp_eq_u64 s[66:67], s[32:33], v[236:237]\n"
    "s_cmp_eq_u64 s[66:67], -1\n"
    "s_cbranch_scc1 PL7x_%=\n"
    "s_sleep 2\n"
    "s_branch PL7_%=\n"
    "PL7x_%=:\n"
    "s_add_u32 s49, s49, 1536\n"
    "s_cmp_lt_u32 s49, 8265728\n"
    "s_cbranch_scc1 EPL_%=\n"
    "EPOK_%=:\n"
    "s_mov_b32 s48, 8292352\n"
    "v_mov_b32 v10, s32\n"
    "v_mov_b32 v11, s33\n"
    "v_mov_b32 v9, 0\n"
    "s_mov_b64 exec, 1\n"
    "buffer_store_dwordx2 v[10:11], v9, s[28:31], s48 offen sc1\n"
    "s_mov_b64 exec, -1\n"
    "END_%=:\n"
    "s_endpgm\n"
    "EPQ_%=:\n"
    "v_and_b32 v9, 31, v1\n"
    "v_min_u32 v9, 23, v9\n"
    "v_lshlrev_b32 v9, 3, v9\n"
    "v_and_b32 v10, 32, v1\n"
    "v_lshl_add_u32 v230, v10, 9, v9\n"
    "s_mov_b32 s49, 8264192\n"
    "buffer_load_dwordx2 v[240:241], v230, s[28:31], s49 offen sc1\n"
    "s_mov_b32 s49, 8262656\n"
    "buffer_load_dwordx2 v[242:243], v230, s[28:31], s49 offen sc1\n"
    "s_mov_b32 s49, 8261120\n"
    "buffer_load_dwordx2 v[244:245], v230, s[28:31], s49 offen sc1\n"
    "s_mov_b32 s49, 8259584\n"
    "buffer_load_dwordx2 v[246:247], v230, s[28:31], s49 offen sc1\n"
    "s_mov_b32 s49, 8258048\n"
    "buffer_load_dwordx2 v[248:249], v230, s[28:31], s49 offen sc1\n"
    "s_branch EPB_%=\n"
    "SPARE2_%=:\n"
    "s_mov_b32 s48, 8292352\n"
    "s_load_dwordx2 s[32:33], s[28:29], s48\n"
    "v_lshrrev_b32 v10, 4, v1\n"
    "v_bfe_u32 v9, v1, 2, 2\n"
    "v_lshl_add_u32 v9, v10, 3, v9\n"
    "v_mul_u32_u24 v9, 160, v9\n"
    "v_add_u32 v9, s42, v9\n"
    "v_and_b32 v10, 3, v1\n"
    "v_lshl_add_u32 v21, v10, 4, v9\n"
    "v_lshl_add_u32 v22, v10, 3, v9\n"
    "s_lshr_b32 s36, s2, 5\n"
    "s_and_b32 s48, s2, 1\n"
    "s_and_b32 s49, s4, 3\n"
    "s_lshl2_add_u32 s6, s48, s49\n"
    "s_and_b32 s48, s2, 4\n"
    "s_lshl1_add_u32 s48, s48, s36\n"
    "s_bfe_u32 s49, s2, 0x10001\n"
    "s_lshl1_add_u32 s64, s48, s49\n"
    "s_and_b32 s59, s3, 1\n"
    "s_lshr_b32 s60, s3, 1\n"
    "s_add_u32 s80, s3, s36\n"
    "s_and_b32 s80, s80, 7\n"
    "s_mul_i32 s50, s64, 192\n"
    "s_mul_i32 s48, s80, 6\n"
    "s_add_u32 s50, s50, s48\n"
    "s_add_u32 s48, s60, 1\n"
    "s_and_b32 s48, s48, 3\n"
    "s_mul_i32 s48, s48, 48\n"
    "s_add_u32 s48, s48, s50\n"
    "s_lshl_b32 s54, s48, 10\n"
    "s_add_u32 s48, s60, 2\n"
    "s_and_b32 s48, s48, 3\n"
    "s_mul_i32 s48, s48, 48\n"
    "s_add_u32 s48, s48, s50\n"
    "s_lshl_b32 s55, s48, 10\n"
    "s_add_u32 s48, s60, 3\n"
    "s_and_b32 s48, s48, 3\n"
    "s_mul_i32 s48, s48, 48\n"
    "s_add_u32 s48, s48, s50\n"
    "s_lshl_b32 s56, s48, 10\n"
    "s_add_u32 s48, s60, 4\n"
    "s_and_b32 s48, s48, 3\n"
    "s_mul_i32 s48, s48, 48\n"
    "s_add_u32 s48, s48, s50\n"
    "s_lshl_b32 s57, s48, 10\n"
    "s_mul_i32 s82, s3, 5120\n"
    "s_xor_b32 s48, s3, 4\n"
    "s_mul_i32 s83, s48, 5120\n"
    "s_add_u32 s48, s3, 2\n"
    "s_and_b32 s48, s48, 7\n"
    "s_mul_i32 s84, s48, 5120\n"
    "s_xor_b32 s48, s48, 1\n"
    "s_mul_i32 s85, s48, 5120\n"
    "s_xor_b32 s48, s3, 1\n"
    "s_mul_i32 s86, s48, 5120\n"
    "s_add_u32 s86, s86, 81920\n"
    "s_lshl2_add_u32 s48, s64, s60\n"
    "s_mul_i32 s48, s48, 20480\n"
    "s_mul_i32 s49, s6, 160\n"
    "s_add_u32 s88, s48, s49\n"
    "s_mul_i32 s49, s59, 96\n"
    "s_add_u32 s87, s88, s49\n"
    "s_lshl3_add_u32 s48, s6, s80\n"
    "s_mul_i32 s58, s48, 30720\n"
    "s_add_u32 s58, s58, 6291456\n"
    "s_lshl_b32 s61, s6, 8\n"
    "s_add_u32 s61, s61, 8290304\n"
    "s_lshl_b32 s48, s59, 12\n"
    "v_add_u32 v240, s48, v7\n"
    "s_lshl_b32 s48, s59, 11\n"
    "s_sub_u32 s48, 1024, s48\n"
    "v_add_u32 v241, s48, v240\n"
    "v_add_u32 v242, s48, v241\n"
    "v_add_u32 v243, s48, v242\n"
    "v_add_u32 v244, s48, v243\n"
    "v_add_u32 v224, s82, v7\n"
    "v_add_u32 v225, 122880, v224\n"
    "v_add_u32 v226, s83, v7\n"
    "v_add_u32 v227, 122880, v226\n"
    "v_add_u32 v228, s84, v7\n"
    "v_add_u32 v229, s85, v7\n"
    "v_add_u32 v233, s86, v7\n"
    "v_and_b32 v9, 15, v1\n"
    "v_mul_u32_u24 v9, 1280, v9\n"
    "v_and_b32 v10, 48, v1\n"
    "v_add_u32 v231, v9, v10\n"
    "v_lshrrev_b32 v10, 1, v10\n"
    "v_add_u32 v232, v9, v10\n"
    "v_add_u32 v8, 4096, v7\n"
    "s_and_b32 s48, s64, 15\n"
    "s_mul_i32 s48, s48, 316\n"
    "s_add_u32 s49, s48, 237\n"
    "s_lshr_b32 s48, s48, 10\n"
    "s_lshr_b32 s49, s49, 10\n"
    "s_sub_u32 s49, s49, s48\n"
    "s_and_b32 s50, s64, 15\n"
    "s_lshr_b32 s48, s64, 4\n"
    "s_lshl_b32 s48, s48, 14\n"
    "s_lshl_b32 s50, s50, 9\n"
    "s_add_u32 s48, s48, s50\n"
    "s_lshl_b32 s49, s49, 3\n"
    "v_and_b32 v9, 31, v1\n"
    "v_min_u32 v9, 23, v9\n"
    "v_lshlrev_b32 v9, 3, v9\n"
    "v_and_b32 v10, 32, v1\n"
    "v_mad_u32_u24 v230, v10, s49, v9\n"
    "v_add_u32 v230, s48, v230\n"
    "s_mul_i32 s48, s3, 24\n"
    "s_add_u32 s48, s48, s39\n"
    "s_mul_i32 s49, s48, 10240\n"
    "s_add_u32 s49, s49, 6291456\n"
    "s_lshl_b32 s48, s3, 5\n"
    "s_add_u32 s48, s48, s39\n"
    "s_lshl_b32 s62, s48, 3\n"
    "s_add_u32 s62, s62, 8290304\n"
    "v_and_b32 v143, 31, v1\n"
    "v_lshlrev_b32 v143, 3, v143\n"
    "s_waitcnt lgkmcnt(0)\n"
    "s_add_u32 s32, s32, 1\n"
    "s_addc_u32 s33, s33, 0\n"
    "s_barrier\n"
    "s_waitcnt vmcnt(5)\n"
    "ds_read_b64_tr_b16 v[32:33], v21 offset:0\n"
    "ds_read_b64_tr_b16 v[34:35], v21 offset:640\n"
    "ds_read_b64_tr_b16 v[36:37], v21 offset:8\n"
    "ds_read_b64_tr_b16 v[38:39], v21 offset:648\n"
    "ds_read_b64_tr_b16 v[40:41], v22 offset:64\n"
    "ds_read_b64_tr_b16 v[42:43], v22 offset:704\n"
    "ds_read_b64_tr_b16 v[44:45], v21 offset:104\n"
    "ds_read_b64_tr_b16 v[46:47], v21 offset:744\n"
    "ds_read_b64_tr_b16 v[48:49], v21 offset:96\n"
    "ds_read_b64_tr_b16 v[50:51], v21 offset:736\n"
    "s_waitcnt lgkmcnt(0)\n"
    "buffer_store_dwordx4 v[32:35], v7, s[28:31], s49 offen offset:0 sc1\n"
    "buffer_store_dwordx4 v[36:39], v7, s[28:31], s49 offen offset:1024 sc1\n"
    "buffer_store_dwordx4 v[40:43], v7, s[28:31], s49 offen offset:2048 sc1\n"
    "buffer_store_dwordx4 v[44:47], v7, s[28:31], s49 offen offset:3072 sc1\n"
    "buffer_store_dwordx4 v[48:51], v8, s[28:31], s49 offen sc1\n"
    "s_add_u32 s49, s49, 5120\n"
    "s_waitcnt vmcnt(5)\n"
    "ds_read_b64_tr_b16 v[52:53], v21 offset:5120\n"
    "ds_read_b64_tr_b16 v[54:55], v21 offset:5760\n"
    "ds_read_b64_tr_b16 v[56:57], v21 offset:5128\n"
    "ds_read_b64_tr_b16 v[58:59], v21 offset:5768\n"
    "ds_read_b64_tr_b16 v[60:61], v22 offset:5184\n"
    "ds_read_b64_tr_b16 v[62:63], v22 offset:5824\n"
    "ds_read_b64_tr_b16 v[64:65], v21 offset:5224\n"
    "ds_read_b64_tr_b16 v[66:67], v21 offset:5864\n"
    "ds_read_b64_tr_b16 v[68:69], v21 offset:5216\n"
    "ds_read_b64_tr_b16 v[70:71], v21 offset:5856\n"
    "s_waitcnt lgkmcnt(0)\n"
    "buffer_store_dwordx4 v[52:55], v7, s[28:31], s49 offen offset:0 sc1\n"
    "buffer_store_dwordx4 v[56:59], v7, s[28:31], s49 offen offset:1024 sc1\n"
    "buffer_store_dwordx4 v[60:63], v7, s[28:31], s49 offen offset:2048 sc1\n"
    "buffer_store_dwordx4 v[64:67], v7, s[28:31], s49 offen offset:3072 sc1\n"
    "buffer_store_dwordx4 v[68:71], v8, s[28:31], s49 offen sc1\n"
    "s_add_u32 s49, s49, 5120\n"
    "s_waitcnt vmcnt(0)\n"
    "v_mov_b32 v10, s32\n"
    "v_mov_b32 v11, s33\n"
    "v_mov_b32 v9, 0\n"
    "s_mov_b64 exec, 1\n"
    "buffer_store_dwordx2 v[10:11], v9, s[28:31], s62 offen sc1\n"
    "s_mov_b64 exec, -1\n"
    "s_barrier\n"
    "s_cmp_lg_u32 s39, 0\n"
    "s_cbranch_scc1 NOPR_%=\n"
    "s_cmp_lg_u32 s3, 0\n"
    "s_cbranch_scc1 PRB_%=\n"
    "s_mov_b32 s50, 8257792\n"
    "v_mov_b32 v9, 0\n"
    "v_mov_b32 v10, 8192\n"
    "s_mov_b32 s41, 0\n"
    "s_mov_b32 s45, 5\n"
    "s_mov_b64 exec, 1\n"
    "PRL_%=:\n"
    "buffer_store_dword v9, v9, s[28:31], s50 offen sc1\n"
    "buffer_store_dword v9, v10, s[28:31], s50 offen sc1\n"
    "s_waitcnt vmcnt(1)\n"
    "s_memtime s[96:97]\n"
    "s_waitcnt vmcnt(0)\n"
    "s_memtime s[98:99]\n"
    "v_xor_b32 v9, 8192, v9\n"
    "v_xor_b32 v10, 8192, v10\n"
    "s_waitcnt lgkmcnt(0)\n"
    "s_sub_u32 s48, s98, s96\n"
    "s_min_u32 s48, s48, 300\n"
    "s_cmp_eq_u32 s45, 5\n"
    "s_cselect_b32 s48, 0, s48\n"
    "s_sub_u32 s41, s48, s41\n"
    "s_sub_u32 s45, s45, 1\n"
    "s_cmp_lg_u32 s45, 0\n"
    "s_cbranch_scc1 PRL_%=\n"
    "s_getreg_b32 s48, hwreg(HW_REG_XCC_ID, 0, 4)\n"
    "s_lshl_b32 s48, s48, 29\n"
    "s_xor_b32 s41, s41, s48\n"
    "s_and_b32 s41, s41, 0x80000000\n"
    "s_xor_b32 s41, s33, s41\n"
    "v_mov_b32 v10, s32\n"
    "v_mov_b32 v11, s41\n"
    "v_mov_b32 v9, 0\n"
    "s_mov_b32 s48, 8292480\n"
    "buffer_store_dwordx2 v[10:11], v9, s[28:31], s48 offen sc1\n"
    "s_mov_b64 exec, -1\n"
    "PRB_%=:\n"
    "s_barrier\n"
    "s_branch PRX_%=\n"
    "NOPR_%=:\n"
    "s_sleep 32\n"
    "PRX_%=:\n"
    "s_mov_b32 s45, 1048576\n"
    "PL8_%=:\n"
    "buffer_load_dwordx2 v[144:145], v143, s[28:31], s61 offen sc1\n"
    "s_sub_u32 s45, s45, 1\n"
    "s_cbranch_scc1 PL8x_%=\n"
    "s_waitcnt vmcnt(0)\n"
    "v_cmp_eq_u64 s[66:67], s[32:33], v[144:145]\n"
    "s_cmp_eq_u64 s[66:67], -1\n"
    "s_cbranch_scc1 PL8x_%=\n"
    "s_sleep 16\n"
    "s_branch PL8_%=\n"
    "PL8x_%=:\n"
    "buffer_load_dwordx4 v[16:19], v240, s[28:31], s58 offen sc1\n"
    "buffer_load_dwordx4 v[20:23], v241, s[28:31], s58 offen sc1\n"
    "buffer_load_dwordx4 v[24:27], v242, s[28:31], s58 offen sc1\n"
    "buffer_load_dwordx4 v[28:31], v243, s[28:31], s58 offen sc1\n"
    "buffer_load_dwordx4 v[32:35], v244, s[28:31], s58 offen sc1\n"
    "s_add_u32 s58, s58, 5120\n"
    "buffer_load_dwordx4 v[36:39], v240, s[28:31], s58 offen sc1\n"
    "buffer_load_dwordx4 v[40:43], v241, s[28:31], s58 offen sc1\n"
    "buffer_load_dwordx4 v[44:47], v242, s[28:31], s58 offen sc1\n"
    "buffer_load_dwordx4 v[48:51], v243, s[28:31], s58 offen sc1\n"
    "buffer_load_dwordx4 v[52:55], v244, s[28:31], s58 offen sc1\n"
    "s_add_u32 s58, s58, 5120\n"
    "buffer_load_dwordx4 v[56:59], v240, s[28:31], s58 offen sc1\n"
    "buffer_load_dwordx4 v[60:63], v241, s[28:31], s58 offen sc1\n"
    "buffer_load_dwordx4 v[64:67], v242, s[28:31], s58 offen sc1\n"
    "buffer_load_dwordx4 v[68:71], v243, s[28:31], s58 offen sc1\n"
    "buffer_load_dwordx4 v[72:75], v244, s[28:31], s58 offen sc1\n"
    "s_add_u32 s58, s58, 5120\n"
    "buffer_load_dwordx4 v[76:79], v240, s[28:31], s58 offen sc1\n"
    "buffer_load_dwordx4 v[80:83], v241, s[28:31], s58 offen sc1\n"
    "buffer_load_dwordx4 v[84:87], v242, s[28:31], s58 offen sc1\n"
    "buffer_load_dwordx4 v[88:91], v243, s[28:31], s58 offen sc1\n"
    "buffer_load_dwordx4 v[92:95], v244, s[28:31], s58 offen sc1\n"
    "s_add_u32 s58, s58, 5120\n"
    "buffer_load_dwordx4 v[96:99], v240, s[28:31], s58 offen sc1\n"
    "buffer_load_dwordx4 v[100:103], v241, s[28:31], s58 offen sc1\n"
    "buffer_load_dwordx4 v[104:107], v242, s[28:31], s58 offen sc1\n"
    "buffer_load_dwordx4 v[108:111], v243, s[28:31], s58 offen sc1\n"
    "buffer_load_dwordx4 v[112:115], v244, s[28:31], s58 offen sc1\n"
    "s_add_u32 s58, s58, 5120\n"
    "buffer_load_dwordx4 v[116:119], v240, s[28:31], s58 offen sc1\n"
    "buffer_load_dwordx4 v[120:123], v241, s[28:31], s58 offen sc1\n"
    "buffer_load_dwordx4 v[124:127], v242, s[28:31], s58 offen sc1\n"
    "buffer_load_dwordx4 v[128:131], v243, s[28:31], s58 offen sc1\n"
    "buffer_load_dwordx4 v[132:135], v244, s[28:31], s58 offen sc1\n"
    "s_mov_b64 vcc, 0\n"
    "s_cmp_lg_u32 s3, 4\n"
    "s_cbranch_scc1 HOK_%=\n"
    "s_sleep 32\n"
    "s_mov_b32 s48, 8292480\n"
    "s_load_dwordx2 s[96:97], s[28:29], s48\n"
    "s_branch HPOLL_%=\n"
    "CODEEND_%=:\n"
    :: "{s[0:1]}"(__builtin_amdgcn_kernarg_segment_ptr()), "{s2}"(blockIdx.x), "{v0}"(threadIdx.x)
    : "v255", "s101", "vcc", "scc", "memory");
}

extern "C" void kernel_launch(void* const* d_in, const int* in_sizes, int n_in, void* d_out, int out_size, void* d_ws, size_t ws_size,
                              hipStream_t stream) {
  const unsigned F = 0x00020000u;
  (void)hipFuncSetAttribute((const void*)glu_fused, hipFuncAttributeMaxDynamicSharedMemorySize, 163840);
  glu_fused<<<256, 512, 163840, stream>>>(d_in[0], 2621440u, F, d_in[1], 1966080u, F, d_in[2], 1966080u, F, d_in[3], 1966080u, F,
                                          d_out, 2621440u, F, d_ws, 8292608u, F);
}
```
